```python
import torch

def _softmax(x, axis=-1):
    return torch.softmax(x, dim=axis)
def _gelu(x):
    return torch.nn.functional.gelu(x, approximate="tanh")
def _silu(x):
    return torch.nn.functional.silu(x)
def _sigmoid(x):
    return torch.sigmoid(x)
def _layernorm(x, axis=-1, eps=1e-5):
    m = x.mean(dim=axis, keepdim=True)
    v = x.var(dim=axis, keepdim=True, unbiased=False)
    return (x - m) / torch.sqrt(v + eps)
def _scatter_add(tgt, idx, src, axis=0):
    return tgt.clone().index_add_(axis, idx, src)
def _segment_sum(src, seg, n):
    out = torch.zeros((n,) + src.shape[1:], dtype=src.dtype)
    return out.index_add_(0, seg, src)
def _pad(x, pw):
    flat = tuple(v for pair in reversed(pw) for v in pair)
    return torch.nn.functional.pad(x, flat)

kernel_name = 'standalone__mha_block_cached__near_ridge'

def setup_inputs():

    _w = (lambda a: (lambda t: t.to(torch.bfloat16) if t.is_floating_point() else t)(torch.as_tensor(a)))
    B, Sq, D = 8, 64, 640
    H, Sk, Dh = 32, 64, 192
    return {
        "x": _w((lambda *s: torch.randn(*s, dtype=torch.bfloat16) * 0.8)(B, Sq, D)),
        "k_cache": _w((lambda *s: torch.randn(*s, dtype=torch.bfloat16) * 0.8)(B, H, Sk, Dh)),
        "v_cache": _w((lambda *s: torch.randn(*s, dtype=torch.bfloat16) * 0.05)(B, H, Sk, Dh)),
        "wq": _w((lambda *s: torch.randn(*s, dtype=torch.bfloat16) * 0.8)(D, H * Dh) * (D ** -0.5)),
        "wo": _w((lambda *s: torch.randn(*s, dtype=torch.bfloat16) * 0.05)(H * Dh, D) * ((H * Dh) ** -0.5)),
    }

def reference(x, k_cache, v_cache, wq, wo):
    B, Sq, D = x.shape
    H, Dh = 32, 192
    q = (lambda x, *d: x.permute(*d))((x @ wq).reshape(B, Sq, H, Dh), 0, 2, 1, 3)
    scores = torch.einsum("bhqd,bhkd->bhqk", q, k_cache) * (Dh ** -0.5)
    p = _softmax(scores, axis=-1)
    o = torch.einsum("bhqk,bhkd->bhqd", p, v_cache)
    return (lambda x, *d: x.permute(*d))(o, 0, 2, 1, 3).reshape(B, Sq, H * Dh) @ wo
```

```cpp
#include <hip/hip_runtime.h>
#include <hip/hip_bf16.h>
typedef unsigned short u16;
__device__ __forceinline__ float b2f(u16 x){ return __uint_as_float(((unsigned)x)<<16); }
__device__ __forceinline__ u16 f2b(float f){ unsigned u=__float_as_uint(f); u += 0x7fffu + ((u>>16)&1u); return (u16)(u>>16); }
constexpr int B=8,SQ=64,D=640,H=32,SK=64,DH=192;
__global__ void k_q(const u16* x,const u16* wq,u16* q){
  int n = blockIdx.x*256+threadIdx.x; int row = blockIdx.y;
  float acc=0; for(int k=0;k<D;k++) acc += b2f(x[row*D+k])*b2f(wq[(size_t)k*(H*DH)+n]);
  q[(size_t)row*(H*DH)+n]=f2b(acc);
}
__global__ void k_attn(const u16* q,const u16* kc,const u16* vc,u16* o){
  int bh = blockIdx.x; int b=bh/H,h=bh%H; int row=blockIdx.y; int t=threadIdx.x;
  __shared__ float p[64];
  const u16* qr = q+(size_t)(b*SQ+row)*(H*DH)+h*DH;
  const u16* kr = kc+((size_t)bh*SK+t)*DH;
  float acc=0; for(int d=0;d<DH;d++) acc+=b2f(qr[d])*b2f(kr[d]);
  float s=b2f(f2b(acc)); s=b2f(f2b(s*0.07216878364870322f));
  float m=s; for(int i=32;i>=1;i>>=1) m=fmaxf(m,__shfl_xor(m,i));
  float e=__expf(s-m); float sum=e; for(int i=32;i>=1;i>>=1) sum+=__shfl_xor(sum,i);
  p[t]=b2f(f2b(e/sum));
  __syncthreads();
  for(int d=t;d<DH;d+=64){ float a=0; for(int k=0;k<SK;k++) a+=p[k]*b2f(vc[((size_t)bh*SK+k)*DH+d]); o[(size_t)(b*SQ+row)*(H*DH)+h*DH+d]=f2b(a);}
}
__global__ void k_o(const u16* o,const u16* wo,u16* out){
  int n = blockIdx.x*64+threadIdx.x; int row=blockIdx.y;
  float acc=0; for(int k=0;k<H*DH;k++) acc+=b2f(o[(size_t)row*(H*DH)+k])*b2f(wo[(size_t)k*D+n]);
  out[row*D+n]=f2b(acc);
}
extern "C" void kernel_launch(void* const* d_in, const int* in_sizes, int n_in,
                              void* d_out, int out_size, void* d_ws, size_t ws_size,
                              hipStream_t stream) {
  const u16* x=(const u16*)d_in[0]; const u16* kc=(const u16*)d_in[1]; const u16* vc=(const u16*)d_in[2];
  const u16* wq=(const u16*)d_in[3]; const u16* wo=(const u16*)d_in[4];
  u16* q=(u16*)d_ws; u16* o=q+(size_t)B*SQ*H*DH;
  k_q<<<dim3(H*DH/256,B*SQ),256,0,stream>>>(x,wq,q);
  k_attn<<<dim3(B*H,SQ),64,0,stream>>>(q,kc,vc,o);
  k_o<<<dim3(D/64,B*SQ),64,0,stream>>>(o,wo,(u16*)d_out);
}
```

```cpp
#include <hip/hip_runtime.h>
#include <hip/hip_bf16.h>
#include <stdint.h>

#define LAS __attribute__((address_space(3)))
#define GAS __attribute__((address_space(1)))
#define DI __device__ __forceinline__
typedef short s16x4 __attribute__((ext_vector_type(4)));
typedef short s16x8 __attribute__((ext_vector_type(8)));
typedef __bf16 bf16x2 __attribute__((ext_vector_type(2)));
typedef __bf16 bf16x8 __attribute__((ext_vector_type(8)));
typedef float f32x2 __attribute__((ext_vector_type(2)));
typedef float f32x4 __attribute__((ext_vector_type(4)));
typedef unsigned u32x2 __attribute__((ext_vector_type(2)));
typedef unsigned u32x4 __attribute__((ext_vector_type(4)));
typedef unsigned long long u64;

constexpr int NB = 8, SQ = 64, DM = 640, NH = 32, SK = 64, DH = 192;
constexpr int NCH = 23;
constexpr unsigned SLOT = 32768, AUX = 24576;
constexpr unsigned PBUF = 131072, MISC = PBUF + 3 * 8192, DUMMY = MISC + 256, LDS_BYTES = MISC + 512 + 4096;
constexpr unsigned L_FULL = 0, L_PROG = 16, L_POWN = 32, L_PFULL = 48, L_PVW = 64, L_TAG = 80;
constexpr unsigned WS_EP = 0;
constexpr unsigned WS_ER = 8192;
constexpr unsigned WS_FP = 16384;
constexpr unsigned WS_FR = 16384 + 32768;
constexpr unsigned WS_P = 262144;
constexpr unsigned WS_R = WS_P + 256 * 8192;
constexpr unsigned WS_END = WS_R + 256 * 40960;
constexpr unsigned WS_DBG = 16u << 20;
constexpr unsigned SPIN_CAP = 1u << 22;

#define STAMPS 0
#define PREFETCH 1

DI unsigned rfl(unsigned x) { return __builtin_amdgcn_readfirstlane(x); }
DI unsigned cvtpk(float lo, float hi) { f32x2 v = {lo, hi}; bf16x2 b = __builtin_convertvector(v, bf16x2); return __builtin_bit_cast(unsigned, b); }
DI bf16x8 pack8(const f32x4& a, const f32x4& b) {
  u32x4 p = {cvtpk(a[0], a[1]), cvtpk(a[2], a[3]), cvtpk(b[0], b[1]), cvtpk(b[2], b[3])};
  return __builtin_bit_cast(bf16x8, p);
}
#define MFMA(a, b, c) __builtin_amdgcn_mfma_f32_16x16x32_bf16((a), (b), (c), 0, 0, 0)
DI bf16x8 lds_b128(unsigned addr) { return *(const LAS bf16x8*)(uintptr_t)addr; }
DI u32x4 lds_u128(unsigned addr) { return *(const volatile LAS u32x4*)(uintptr_t)addr; }
DI unsigned lds_u32(unsigned addr) { return *(const volatile LAS unsigned*)(uintptr_t)addr; }
DI void lds_st32(unsigned addr, unsigned v) { *(volatile LAS unsigned*)(uintptr_t)addr = v; }
DI bf16x8 lds_tr(unsigned a0, unsigned a1) {
  s16x4 lo = __builtin_amdgcn_ds_read_tr16_b64_v4i16((LAS s16x4*)(uintptr_t)a0);
  s16x4 hi = __builtin_amdgcn_ds_read_tr16_b64_v4i16((LAS s16x4*)(uintptr_t)a1);
  return __builtin_bit_cast(bf16x8, __builtin_shufflevector(lo, hi, 0, 1, 2, 3, 4, 5, 6, 7));
}
DI bf16x8 lds_2b64(unsigned a0, unsigned a1) {
  s16x4 lo = *(const LAS s16x4*)(uintptr_t)a0;
  s16x4 hi = *(const LAS s16x4*)(uintptr_t)a1;
  return __builtin_bit_cast(bf16x8, __builtin_shufflevector(lo, hi, 0, 1, 2, 3, 4, 5, 6, 7));
}
#define CBAR() asm volatile("" ::: "memory")
DI void dma16(__amdgpu_buffer_rsrc_t r, unsigned lds, unsigned voff, unsigned soff) {
  __builtin_amdgcn_raw_ptr_buffer_load_lds(r, (LAS void*)(uintptr_t)lds, 16, voff, soff, 0, 0);
}
DI void touch(__amdgpu_buffer_rsrc_t r, unsigned voff, unsigned soff) {
  __builtin_amdgcn_raw_ptr_buffer_load_lds(r, (LAS void*)(uintptr_t)DUMMY, 4, voff, soff, 0, 0);
}
DI unsigned opaque(unsigned x) { asm volatile("" : "+v"(x)); return x; }
DI void wait_vm(unsigned n) {
  if (n >= 36) asm volatile("s_waitcnt vmcnt(36)" ::: "memory");
  else if (n >= 24) asm volatile("s_waitcnt vmcnt(24)" ::: "memory");
  else if (n >= 22) asm volatile("s_waitcnt vmcnt(22)" ::: "memory");
  else if (n >= 20) asm volatile("s_waitcnt vmcnt(20)" ::: "memory");
  else if (n >= 16) asm volatile("s_waitcnt vmcnt(16)" ::: "memory");
  else if (n >= 10) asm volatile("s_waitcnt vmcnt(10)" ::: "memory");
  else if (n >= 8) asm volatile("s_waitcnt vmcnt(8)" ::: "memory");
  else asm volatile("s_waitcnt vmcnt(0)" ::: "memory");
}
DI void lds_wait4(unsigned addr, unsigned need, bool sleep) {
  _Pragma("nounroll") for (unsigned sp = 0; sp < SPIN_CAP; ++sp) {
    u32x4 v = lds_u128(addr);
    unsigned m = min(min(v[0], v[1]), min(v[2], v[3]));
    if (rfl(m) >= need) break;
    if (sleep) __builtin_amdgcn_s_sleep(1);
  }
  CBAR();
}
DI void lds_wait3(unsigned addr, unsigned need) {
  _Pragma("nounroll") for (unsigned sp = 0; sp < SPIN_CAP; ++sp) {
    u32x4 v = lds_u128(addr);
    unsigned m = min(min(v[0], v[1]), v[2]);
    if (rfl(m) >= need) break;
  }
  CBAR();
}
DI void lds_wait1(unsigned addr, unsigned need, bool sleep) {
  _Pragma("nounroll") for (unsigned sp = 0; sp < SPIN_CAP; ++sp) {
    if (rfl(lds_u32(addr)) >= need) break;
    if (sleep) __builtin_amdgcn_s_sleep(1);
  }
  CBAR();
}
DI u64 next_tag(u64 e) { return e * 6364136223846793005ull + 1442695040888963407ull; }
DI u64 ld_sc1_64(const void* p) { return __hip_atomic_load((const GAS u64*)p, __ATOMIC_RELAXED, __HIP_MEMORY_SCOPE_AGENT); }
DI void st_sc1_64(void* p, u64 v) { __hip_atomic_store((GAS u64*)p, v, __ATOMIC_RELAXED, __HIP_MEMORY_SCOPE_AGENT); }
#define STAMP(i) do {} while (0)
#define STAMP_BEGIN() do {} while (0)
#define STAMP_END() do {} while (0)

struct Geo { unsigned b, g, j, h, c0; };

template <bool W> DI unsigned npieces(unsigned c) {
  if (c >= NCH) return 0;
  if (!W) return c == 10 ? 0u : 8u;
  return c <= 10 ? 12u : 10u;
}
template <bool W>
DI void dma_wave(const Geo& G, unsigned dw, const void* x, const void* kc, const void* vc, const void* wq, const void* wo, unsigned lane, char* ws, unsigned wave) {
  __amdgpu_buffer_rsrc_t rA, rK, rB;
  unsigned vA[6], vK[6], vB[5];
  if (W) {
    rA = __builtin_amdgcn_make_buffer_rsrc((void*)wq, 0, DM * NH * DH * 2, 0x00020000);
    rK = __builtin_amdgcn_make_buffer_rsrc((void*)kc, 0, NB * NH * SK * DH * 2, 0x00020000);
    rB = __builtin_amdgcn_make_buffer_rsrc((void*)wo, 0, DM * NH * DH * 2, 0x00020000);
#pragma unroll
    for (int m = 0; m < 6; ++m) {
      unsigned by = 1024 * m + 16 * lane, r = by / 384, c = (by % 384) >> 4;
      unsigned fq = (r & 2) | ((r >> 1) & 4);
      vA[m] = r * 12288 + ((c ^ fq) << 4);
      unsigned fk = (r >> 1) & 7;
      vK[m] = r * 384 + ((c ^ fk) << 4);
    }
#pragma unroll
    for (int m = 0; m < 5; ++m) {
      unsigned by = 1024 * m + 16 * lane, r = by / 320, c = (by % 320) >> 4;
      unsigned f = (r >> 2) & 2;
      vB[m] = r * 1280 + ((c ^ f) << 4);
    }
  } else {
    rA = __builtin_amdgcn_make_buffer_rsrc((void*)x, 0, NB * SQ * DM * 2, 0x00020000);
    rK = rA;
    rB = __builtin_amdgcn_make_buffer_rsrc((void*)vc, 0, NB * NH * SK * DH * 2, 0x00020000);
#pragma unroll
    for (int m = 0; m < 2; ++m) {
      unsigned r = lane >> 3, c = lane & 7, f = (4 * m + (r >> 1)) & 7;
      vA[m] = r * 1280 + ((c ^ f) << 4);
      vB[m] = r * 384 + ((c ^ f) << 4);
    }
  }
#pragma nounroll
  for (unsigned n = 0; n < NCH + 2; ++n) {
    if (n < NCH) {
      if (n >= 4) lds_wait4(MISC + L_PROG, n - 3, true);
      STAMP(16 + 4 * n);
      const unsigned slot = (n & 3) * SLOT;
      if (n < 10) {
        unsigned kcx = G.c0 + n; if (kcx >= 10) kcx -= 10;
        if (W) {
          const unsigned so = (kcx * 64 + dw * 32) * 12288 + G.h * 384;
#pragma unroll
          for (int p = 0; p < 12; ++p) dma16(rA, slot + dw * 12288 + p * 1024, vA[p % 6], so + (p / 6) * 16 * 12288);
        } else {
          const unsigned so = G.b * 64 * 1280 + kcx * 128;
#pragma unroll
          for (int p = 0; p < 8; ++p) dma16(rA, slot + AUX + p * 1024, vA[p & 1], so + p * 8 * 1280);
        }
      } else if (n == 10) {
        if (W) {
          const unsigned so = ((G.b * 32 + G.h) * 64 + dw * 32) * 384;
#pragma unroll
          for (int p = 0; p < 12; ++p) dma16(rK, slot + dw * 12288 + p * 1024, vK[p % 6], so + (p / 6) * 16 * 384);
        }
      } else {
        const unsigned cb = n - 11, u = (cb * 11) >> 5, dc = cb - 3 * u, hh = (G.j + u) & 3;
        if (W) {
          const unsigned so = (768 * G.g + 192 * hh + 64 * dc + dw * 32) * 1280 + 320 * G.j;
#pragma unroll
          for (int p = 0; p < 10; ++p) dma16(rB, slot + dw * 10240 + p * 1024, vB[p % 5], so + (p / 5) * 16 * 1280);
        } else {
          const unsigned so = (G.b * 32 + 4 * G.g + hh) * 64 * 384 + dc * 128;
#pragma unroll
          for (int p = 0; p < 8; ++p) dma16(rB, slot + AUX + p * 1024, vB[p & 1], so + p * 8 * 384);
        }
      }
    }
    STAMP(17 + 4 * n);
    if (n == 3) {
      STAMP(1);
      asm volatile("s_barrier" ::: "memory");
      wait_vm(npieces<W>(1) + npieces<W>(2) + npieces<W>(3));
      lds_st32(MISC + L_FULL + 4 * dw, 1);
      STAMP(2);
    }
    if (n >= 3 && n - 2 < NCH) {
      wait_vm(npieces<W>(n - 1) + npieces<W>(n));
      lds_st32(MISC + L_FULL + 4 * dw, n - 1);
      STAMP(18 + 4 * n);
    }
  }
  STAMP(3);
}

DI void reduce_tile(const Geo& G, unsigned e5, u64 tagR, char* ws, void* out, unsigned lane, bool upd) {
  const unsigned it = G.g >> 1, nt = 5 * (G.g & 1) + e5;
  const unsigned pw = nt < 3 ? 0u : nt < 6 ? 1u : nt < 8 ? 2u : 3u;
  const char* fl = ws + WS_FR + (size_t)((((G.b * 4 + G.j) * 8 + (lane & 7)) * 4 + pw) * 128);
  _Pragma("nounroll") for (unsigned sp = 0; sp < SPIN_CAP; ++sp) {
    u64 v = ld_sc1_64(fl);
    if (__all(v == tagR)) break;
    __builtin_amdgcn_s_sleep(1);
  }
  CBAR();
  __amdgpu_buffer_rsrc_t rR = __builtin_amdgcn_make_buffer_rsrc((void*)(ws + WS_R), 0, 256 * 40960, 0x00020000);
  const unsigned base = ((G.b * 4 + G.j) * 8) * 40960 + (it * 10 + nt) * 1024;
  u32x4 v[8];
#pragma unroll
  for (int s = 0; s < 8; ++s) v[s] = __builtin_amdgcn_raw_buffer_load_b128(rR, lane * 16, base + s * 40960, 16);
  f32x4 a = __builtin_bit_cast(f32x4, v[0]);
#pragma unroll
  for (int s = 1; s < 8; ++s) a += __builtin_bit_cast(f32x4, v[s]);
  u32x2 o = {cvtpk(a[0], a[1]), cvtpk(a[2], a[3])};
  __amdgpu_buffer_rsrc_t rO = __builtin_amdgcn_make_buffer_rsrc(out, 0, NB * SQ * DM * 2, 0x00020000);
  const unsigned row = G.b * 64 + 16 * it + (lane & 15), col = 160 * G.j + 16 * nt + 4 * (lane >> 4);
  __builtin_amdgcn_raw_buffer_store_b64(o, rO, (row * DM + col) * 2, 0, 0);
  if (upd && lane == 0) st_sc1_64(ws + WS_ER + (G.b * 4 + G.j) * 128, tagR);
}

__global__ void __launch_bounds__(512, 2)
mha_fused(const void* __restrict__ x, const void* __restrict__ kc, const void* __restrict__ vc, const void* __restrict__ wq,
          const void* __restrict__ wo, void* __restrict__ out, char* __restrict__ ws) {
  const unsigned tid = threadIdx.x, lane = tid & 63, wave = rfl(tid >> 6);
  Geo G;
  { const unsigned bid = blockIdx.x; G.g = bid & 7; const unsigned idx = bid >> 3; G.j = idx & 3; G.b = idx >> 2; G.h = 4 * G.g + G.j;
    const unsigned fb = (G.b * 10) >> 3, oj = (G.j * 10 + 2) >> 2;
    unsigned c0 = fb + oj; if (c0 >= 10) c0 -= 10; G.c0 = c0; }
  STAMP_BEGIN();
  if (wave >= 4 && wave < 7) {
    if (wave < 6) dma_wave<true>(G, wave - 4, x, kc, vc, wq, wo, lane, ws, wave);
    else dma_wave<false>(G, 2, x, kc, vc, wq, wo, lane, ws, wave);
    lds_wait1(MISC + L_PFULL, 3, true);
    const u64 tagR = (u64)lds_u32(MISC + L_TAG) | ((u64)lds_u32(MISC + L_TAG + 4) << 32);
#pragma nounroll
    for (unsigned e5 = wave - 4; e5 < 5; e5 += 4) reduce_tile(G, e5, tagR, ws, out, lane, e5 == 0 && G.g == 0);
    STAMP(5);
  } else if (wave == 7) {
    const u64 eP = ld_sc1_64(ws + WS_EP + (G.b * 8 + G.g) * 128);
    const u64 eR = ld_sc1_64(ws + WS_ER + (G.b * 4 + G.j) * 128);
    {
      __amdgpu_buffer_rsrc_t rK = __builtin_amdgcn_make_buffer_rsrc((void*)kc, 0, NB * NH * SK * DH * 2, 0x00020000);
#pragma unroll
      for (int i = 0; i < 3; ++i) touch(rK, lane * 128, (G.b * 32 + G.h) * 24576 + i * 8192);
    }
    asm volatile("s_barrier" ::: "memory");
    const u64 tagP = next_tag(eP), tagR = next_tag(eR);
    if (lane == 0) { lds_st32(MISC + L_TAG, (unsigned)tagR); lds_st32(MISC + L_TAG + 4, (unsigned)(tagR >> 32)); }
    lds_wait1(MISC + L_PROG, 2, true);
    { __amdgpu_buffer_rsrc_t rV = __builtin_amdgcn_make_buffer_rsrc((void*)vc, 0, NB * NH * SK * DH * 2, 0x00020000);
#pragma unroll
      for (int i = 0; i < 3; ++i) touch(rV, lane * 128, (G.b * 32 + G.h) * 24576 + i * 8192);
      __amdgpu_buffer_rsrc_t rW = __builtin_amdgcn_make_buffer_rsrc((void*)wo, 0, DM * NH * DH * 2, 0x00020000);
#pragma unroll
      for (int i = 0; i < 5; ++i) { unsigned n = i * 64 + lane; n = n < 288 ? n : 287; unsigned r = n / 3, l = n - 3 * r;
        touch(rW, r * 1280 + l * 128, (768 * G.g + 96 * G.b) * 1280 + ((320 * G.j) & ~127u)); }
    }
    STAMP(1);
    lds_wait4(MISC + L_POWN, 1, true);
    STAMP(2);
    __amdgpu_buffer_rsrc_t rP = __builtin_amdgcn_make_buffer_rsrc((void*)(ws + WS_P), 0, 256 * 8192, 0x00020000);
    {
      u32x4 f[8];
#pragma unroll
      for (int i = 0; i < 8; ++i) f[i] = *(const LAS u32x4*)(uintptr_t)(PBUF + i * 1024 + lane * 16);
#pragma unroll
      for (int i = 0; i < 8; ++i) __builtin_amdgcn_raw_buffer_store_b128(f[i], rP, lane * 16, (G.b * 32 + G.h) * 8192 + i * 1024, 16);
      asm volatile("s_waitcnt vmcnt(0)" ::: "memory");
      if (lane == 0) st_sc1_64(ws + WS_FP + (G.b * 32 + G.h) * 128, tagP);
    }
    STAMP(3);
    { const char* fl = ws + WS_FP + (size_t)(G.b * 32 + 4 * G.g + (lane & 3)) * 128;
      _Pragma("nounroll") for (unsigned sp = 0; sp < SPIN_CAP; ++sp) {
        u64 v = ld_sc1_64(fl);
        if (__all(v == tagP)) break;
        __builtin_amdgcn_s_sleep(1);
      }
      CBAR(); }
    STAMP(4);
    if (G.j == 0 && lane == 0) st_sc1_64(ws + WS_EP + (G.b * 8 + G.g) * 128, tagP);
    {
      u32x4 f[3][8];
#pragma unroll
      for (int u = 0; u < 3; ++u) {
        const unsigned hh = (G.j + u + 1) & 3;
#pragma unroll
        for (int i = 0; i < 8; ++i) f[u][i] = __builtin_amdgcn_raw_buffer_load_b128(rP, lane * 16, (G.b * 32 + 4 * G.g + hh) * 8192 + i * 1024, 16);
      }
#pragma unroll
      for (int u = 0; u < 2; ++u)
#pragma unroll
        for (int i = 0; i < 8; ++i) *(LAS u32x4*)(uintptr_t)(PBUF + (u + 1) * 8192 + i * 1024 + lane * 16) = f[u][i];
      CBAR();
      lds_st32(MISC + L_PFULL, 3);
      lds_wait4(MISC + L_PVW, 1, true);
#pragma unroll
      for (int i = 0; i < 8; ++i) *(LAS u32x4*)(uintptr_t)(PBUF + i * 1024 + lane * 16) = f[2][i];
      CBAR();
      lds_st32(MISC + L_PFULL, 4);
    }
    STAMP(5);
    reduce_tile(G, 3, tagR, ws, out, lane, false);
    STAMP(6);
  } else {
    if (tid < 32) lds_st32(MISC + 4 * tid, 0);
    asm volatile("s_waitcnt lgkmcnt(0)\n\ts_barrier" ::: "memory");
    const unsigned i16 = lane & 15, g4 = lane >> 4, q = i16 >> 2, p = lane & 3;
    f32x4 acc[12];
#pragma unroll
    for (int t = 0; t < 12; ++t) acc[t] = (f32x4){0.f, 0.f, 0.f, 0.f};
    const unsigned xa0 = AUX + (16 * wave + i16) * 128 + ((g4 ^ (i16 >> 1)) << 4), xa1 = xa0 ^ 64;
    unsigned wa[4];
    { const unsigned phi = ((q >> 1) & 1) | ((g4 & 1) << 1);
#pragma unroll
      for (int tl = 0; tl < 4; ++tl) wa[tl] = (8 * g4 + q) * 384 + 32 * (tl ^ phi) + 16 * (p >> 1) + 8 * (p & 1); }
#pragma nounroll
    for (unsigned c = 0; c < 10; ++c) {
      lds_wait3(MISC + L_FULL, c + 1);
      STAMP(16 + 4 * c);
      const unsigned slot = (c & 3) * SLOT;
      unsigned wb[4];
#pragma unroll
      for (int tl = 0; tl < 4; ++tl) wb[tl] = opaque(slot + wa[tl]);
#pragma unroll
      for (int ks = 0; ks < 2; ++ks) {
        const bf16x8 xf = lds_b128(slot + (ks ? xa1 : xa0));
#pragma unroll
        for (int t = 0; t < 12; ++t) {
          const unsigned a = wb[t & 3] + 128 * (t >> 2) + ks * 32 * 384;
          acc[t] = MFMA(lds_tr(a, a + 4 * 384), xf, acc[t]);
        }
      }
      CBAR();
      lds_st32(MISC + L_PROG + 4 * wave, c + 1);
      STAMP(17 + 4 * c);
    }
    STAMP(2);
    bf16x8 qf[6];
#pragma unroll
    for (int s = 0; s < 6; ++s) qf[s] = pack8(acc[2 * s], acc[2 * s + 1]);
    lds_wait3(MISC + L_FULL, 11);
    STAMP(3);
    f32x4 st[4];
    {
      const unsigned slot = (10 & 3) * SLOT, fk = i16 >> 1;
      unsigned ka[2][2];
#pragma unroll
      for (int so = 0; so < 2; ++so)
#pragma unroll
        for (int e = 0; e < 2; ++e) ka[so][e] = opaque(slot + i16 * 384 + (((4 * so + 2 * e + (g4 >> 1)) ^ fk) << 4) + 8 * (g4 & 1));
#pragma unroll
      for (int kt = 0; kt < 4; ++kt) {
        st[kt] = (f32x4){0.f, 0.f, 0.f, 0.f};
#pragma unroll
        for (int s = 0; s < 6; ++s) {
          const unsigned o = kt * 16 * 384 + 128 * (s >> 1);
          st[kt] = MFMA(lds_2b64(ka[s & 1][0] + o, ka[s & 1][1] + o), qf[s], st[kt]);
        }
      }
    }
    CBAR();
    lds_st32(MISC + L_PROG + 4 * wave, 11);
    {
      float m = -3.0e38f;
#pragma unroll
      for (int kt = 0; kt < 4; ++kt)
#pragma unroll
        for (int r = 0; r < 4; r += 2) {
          unsigned u = cvtpk(st[kt][r], st[kt][r + 1]);
          float a = __uint_as_float(u << 16) * 0.07216878364870322f, bb = __uint_as_float(u & 0xffff0000u) * 0.07216878364870322f;
          u = cvtpk(a, bb);
          a = __uint_as_float(u << 16); bb = __uint_as_float(u & 0xffff0000u);
          st[kt][r] = a; st[kt][r + 1] = bb;
          m = fmaxf(m, fmaxf(a, bb));
        }
      { auto r1 = __builtin_amdgcn_permlane16_swap(__float_as_uint(m), __float_as_uint(m), false, false);
        m = fmaxf(__uint_as_float(r1[0]), __uint_as_float(r1[1]));
        auto r2 = __builtin_amdgcn_permlane32_swap(__float_as_uint(m), __float_as_uint(m), false, false);
        m = fmaxf(__uint_as_float(r2[0]), __uint_as_float(r2[1])); }
      float sum = 0.f;
      const float ml = m * 1.4426950408889634f;
#pragma unroll
      for (int kt = 0; kt < 4; ++kt)
#pragma unroll
        for (int r = 0; r < 4; ++r) { const float e = __builtin_amdgcn_exp2f(st[kt][r] * 1.4426950408889634f - ml); st[kt][r] = e; sum += e; }
      { auto r1 = __builtin_amdgcn_permlane16_swap(__float_as_uint(sum), __float_as_uint(sum), false, false);
        sum = __uint_as_float(r1[0]) + __uint_as_float(r1[1]);
        auto r2 = __builtin_amdgcn_permlane32_swap(__float_as_uint(sum), __float_as_uint(sum), false, false);
        sum = __uint_as_float(r2[0]) + __uint_as_float(r2[1]); }
      const float inv = 1.0f / sum;
#pragma unroll
      for (int kt = 0; kt < 4; ++kt) st[kt] *= inv;
#pragma unroll
      for (int s2 = 0; s2 < 2; ++s2)
        *(LAS bf16x8*)(uintptr_t)(PBUF + (2 * wave + s2) * 1024 + lane * 16) = pack8(st[2 * s2], st[2 * s2 + 1]);
      CBAR();
      lds_st32(MISC + L_POWN + 4 * wave, 1);
    }
    STAMP(4);
    const unsigned ntb = wave == 3 ? 7u : 3u * wave;
    unsigned va0 = AUX + i16 * 128 + ((g4 ^ (i16 >> 1)) << 4), va1 = va0 ^ 64;
    unsigned ba[3];
#pragma unroll
    for (int e = 0; e < 3; ++e) ba[e] = (8 * g4 + q) * 320 + 32 * ((ntb + e) ^ (g4 & 1)) + 16 * (p >> 1) + 8 * (p & 1);
    f32x4 oa[4][3];
#pragma unroll
    for (int it = 0; it < 4; ++it)
#pragma unroll
      for (int e = 0; e < 3; ++e) oa[it][e] = (f32x4){0.f, 0.f, 0.f, 0.f};
    auto pvw = [&](unsigned buf, const bf16x8 (&pk)[2][3]) {
      const unsigned pb = opaque(PBUF + buf * 8192 + lane * 16);
#pragma unroll
      for (int it = 0; it < 4; ++it)
#pragma unroll
        for (int s2 = 0; s2 < 2; ++s2) {
          const bf16x8 pf = lds_b128(pb + (2 * it + s2) * 1024);
#pragma unroll
          for (int e = 0; e < 3; ++e) oa[it][e] = MFMA(pk[s2][e], pf, oa[it][e]);
        }
    };
    bf16x8 pkA[2][3];
#pragma unroll
    for (int s2 = 0; s2 < 2; ++s2)
#pragma unroll
      for (int e = 0; e < 3; ++e) pkA[s2][e] = qf[0];
#pragma nounroll
    for (unsigned u = 0; u < 4; ++u) {
      f32x4 vw[4][3];
#pragma unroll
      for (int kt = 0; kt < 4; ++kt)
#pragma unroll
        for (int e = 0; e < 3; ++e) vw[kt][e] = (f32x4){0.f, 0.f, 0.f, 0.f};
#pragma nounroll
      for (unsigned dc = 0; dc < 3; ++dc) {
        const unsigned c = 11 + 3 * u + dc;
        lds_wait3(MISC + L_FULL, c + 1);
        STAMP(16 + 4 * c);
        const unsigned slot = (c & 3) * SLOT;
        unsigned bb[3];
#pragma unroll
        for (int e = 0; e < 3; ++e) bb[e] = opaque(slot + ba[e]);
        const unsigned vv0 = opaque(slot + va0), vv1 = opaque(slot + va1);
#pragma unroll
        for (int ks = 0; ks < 2; ++ks) {
          bf16x8 bf[3];
#pragma unroll
          for (int e = 0; e < 3; ++e) { const unsigned a = bb[e] + ks * 32 * 320; bf[e] = lds_tr(a, a + 4 * 320); }
#pragma unroll
          for (int kt = 0; kt < 4; ++kt) {
            const bf16x8 vf = lds_b128((ks ? vv1 : vv0) + kt * 2048);
#pragma unroll
            for (int e = 0; e < 3; ++e) vw[kt][e] = MFMA(vf, bf[e], vw[kt][e]);
          }
        }
        CBAR();
        lds_st32(MISC + L_PROG + 4 * wave, c + 1);
        STAMP(17 + 4 * c);
      }
      bf16x8 pk[2][3];
#pragma unroll
      for (int s2 = 0; s2 < 2; ++s2)
#pragma unroll
        for (int e = 0; e < 3; ++e) pk[s2][e] = pack8(vw[2 * s2][e], vw[2 * s2 + 1][e]);
      STAMP(5 + u);
      if (u == 1) {
#pragma unroll
        for (int s2 = 0; s2 < 2; ++s2)
#pragma unroll
          for (int e = 0; e < 3; ++e) pkA[s2][e] = pk[s2][e];
      } else {
        if (u == 0) lds_wait4(MISC + L_POWN, 1, false);
        else lds_wait1(MISC + L_PFULL, u + 1, false);
        if (u == 2) pvw(1, pkA);
        pvw(u == 2 ? 2u : 0u, pk);
        if (u == 0) { CBAR(); lds_st32(MISC + L_PVW + 4 * wave, 1); }
      }
    }
    STAMP(9);
    const u64 tagR = (u64)lds_u32(MISC + L_TAG) | ((u64)lds_u32(MISC + L_TAG + 4) << 32);
    __amdgpu_buffer_rsrc_t rR = __builtin_amdgcn_make_buffer_rsrc((void*)(ws + WS_R), 0, 256 * 40960, 0x00020000);
    const unsigned base = ((G.b * 4 + G.j) * 8 + G.g) * 40960;
#pragma unroll
    for (int e = 0; e < 3; ++e) {
      const bool keep = wave < 2 || (wave == 2 ? e < 2 : e > 0);
      if (keep) {
#pragma unroll
        for (int it = 0; it < 4; ++it)
          __builtin_amdgcn_raw_buffer_store_b128(__builtin_bit_cast(u32x4, oa[it][e]), rR, lane * 16, base + (it * 10 + ntb + e) * 1024, 16);
      }
    }
    asm volatile("s_waitcnt vmcnt(0)" ::: "memory");
    if (lane == 0) st_sc1_64(ws + WS_FR + (size_t)((((G.b * 4 + G.j) * 8 + G.g) * 4 + wave) * 128), tagR);
    STAMP(10);
  }
  STAMP_END();
}

extern "C" void kernel_launch(void* const* d_in, const int* in_sizes, int n_in, void* d_out, int out_size, void* d_ws, size_t ws_size,
                              hipStream_t stream) {
  (void)in_sizes; (void)n_in; (void)out_size; (void)ws_size;
  hipFuncSetAttribute((const void*)mha_fused, hipFuncAttributeMaxDynamicSharedMemorySize, LDS_BYTES);
  mha_fused<<<256, 512, LDS_BYTES, stream>>>(d_in[0], d_in[1], d_in[2], d_in[3], d_in[4], d_out, (char*)d_ws);
}
```

```cpp
#include <hip/hip_runtime.h>
#include <hip/hip_bf16.h>
#include <stdint.h>

#define LAS __attribute__((address_space(3)))
#define GAS __attribute__((address_space(1)))
#define DI __device__ __forceinline__
typedef short s16x4 __attribute__((ext_vector_type(4)));
typedef short s16x8 __attribute__((ext_vector_type(8)));
typedef __bf16 bf16x2 __attribute__((ext_vector_type(2)));
typedef __bf16 bf16x8 __attribute__((ext_vector_type(8)));
typedef float f32x2 __attribute__((ext_vector_type(2)));
typedef float f32x4 __attribute__((ext_vector_type(4)));
typedef unsigned u32x2 __attribute__((ext_vector_type(2)));
typedef unsigned u32x4 __attribute__((ext_vector_type(4)));
typedef unsigned long long u64;

constexpr int NB = 8, SQ = 64, DM = 640, NH = 32, SK = 64, DH = 192;
constexpr int NCH = 23;
constexpr unsigned SLOT = 32768, AUX = 24576;
constexpr unsigned PBUF = 131072, MISC = PBUF + 3 * 8192, LDS_USED = MISC + 256;
constexpr unsigned L_FULL = 0, L_PROG = 16, L_POWN = 32, L_PFULL = 48, L_PVW = 64, L_TAG = 80, L_QD = 96;
constexpr unsigned WS_EP = 0;
constexpr unsigned WS_ER = 8192;
constexpr unsigned WS_FP = 16384;
constexpr unsigned WS_FR = 16384 + 32768;
constexpr unsigned WS_P = 262144;
constexpr unsigned WS_R = WS_P + 256 * 8192;
constexpr unsigned WS_DBG = 16u << 20;
constexpr unsigned SPIN_CAP = 1u << 16;

#define STAMPS 0
#define MODE 0
#define ROTATE 1
#define IN_AUX 0
#define ST_AUX 16
#define LD_AUX 16
constexpr unsigned LDS_BYTES = LDS_USED + (STAMPS ? 4096 : 0);

DI unsigned rfl(unsigned x) { return __builtin_amdgcn_readfirstlane(x); }
DI unsigned cvtpk(float lo, float hi) { f32x2 v = {lo, hi}; bf16x2 b = __builtin_convertvector(v, bf16x2); return __builtin_bit_cast(unsigned, b); }
DI bf16x8 pack8(const f32x4& a, const f32x4& b) {
  u32x4 p = {cvtpk(a[0], a[1]), cvtpk(a[2], a[3]), cvtpk(b[0], b[1]), cvtpk(b[2], b[3])};
  return __builtin_bit_cast(bf16x8, p);
}
#define MFMA(a, b, c) __builtin_amdgcn_mfma_f32_16x16x32_bf16((a), (b), (c), 0, 0, 0)
DI bf16x8 lds_b128(unsigned addr) { return *(const LAS bf16x8*)(uintptr_t)addr; }
DI u32x4 lds_u128(unsigned addr) { return *(const volatile LAS u32x4*)(uintptr_t)addr; }
DI unsigned lds_u32(unsigned addr) { return *(const volatile LAS unsigned*)(uintptr_t)addr; }
DI void lds_st32(unsigned addr, unsigned v) { *(volatile LAS unsigned*)(uintptr_t)addr = v; }
DI bf16x8 lds_tr(unsigned a0, unsigned a1) {
  s16x4 lo = __builtin_amdgcn_ds_read_tr16_b64_v4i16((LAS s16x4*)(uintptr_t)a0);
  s16x4 hi = __builtin_amdgcn_ds_read_tr16_b64_v4i16((LAS s16x4*)(uintptr_t)a1);
  return __builtin_bit_cast(bf16x8, __builtin_shufflevector(lo, hi, 0, 1, 2, 3, 4, 5, 6, 7));
}
DI bf16x8 lds_2b64(unsigned a0, unsigned a1) {
  s16x4 lo = *(const LAS s16x4*)(uintptr_t)a0;
  s16x4 hi = *(const LAS s16x4*)(uintptr_t)a1;
  return __builtin_bit_cast(bf16x8, __builtin_shufflevector(lo, hi, 0, 1, 2, 3, 4, 5, 6, 7));
}
#define CBAR() asm volatile("" ::: "memory")
#define SB0() __builtin_amdgcn_sched_barrier(0)
DI void dma16(__amdgpu_buffer_rsrc_t r, unsigned lds, unsigned voff, unsigned soff) {
  __builtin_amdgcn_raw_ptr_buffer_load_lds(r, (LAS void*)(uintptr_t)lds, 16, voff, soff, 0, IN_AUX);
}
DI unsigned opaque(unsigned x) { asm volatile("" : "+v"(x)); return x; }
DI unsigned min4(const u32x4& v) { return min(min(v[0], v[1]), min(v[2], v[3])); }
DI unsigned min3v(const u32x4& v) { return min(min(v[0], v[1]), v[2]); }
DI void lds_wait4(unsigned addr, unsigned need, bool sleep) {
#pragma nounroll
  for (unsigned sp = 0; sp < SPIN_CAP; ++sp) {
    if (rfl(min4(lds_u128(addr))) >= need) break;
    if (sleep) __builtin_amdgcn_s_sleep(1);
  }
  CBAR();
}
DI void lds_wait3(unsigned addr, unsigned need) {
  if (MODE == 1) return;
#pragma nounroll
  for (unsigned sp = 0; sp < SPIN_CAP; ++sp) {
    if (rfl(min3v(lds_u128(addr))) >= need) break;
  }
  CBAR();
}
DI void lds_wait1(unsigned addr, unsigned need, bool sleep) {
#pragma nounroll
  for (unsigned sp = 0; sp < SPIN_CAP; ++sp) {
    if (rfl(lds_u32(addr)) >= need) break;
    if (sleep) __builtin_amdgcn_s_sleep(1);
  }
  CBAR();
}
DI u64 next_tag(u64 e) { return e * 6364136223846793005ull + 1442695040888963407ull; }
DI u64 ld_sc1_64(const void* p) { return __hip_atomic_load((const GAS u64*)p, __ATOMIC_RELAXED, __HIP_MEMORY_SCOPE_AGENT); }
DI void st_sc1_64(void* p, u64 v) { __hip_atomic_store((GAS u64*)p, v, __ATOMIC_RELAXED, __HIP_MEMORY_SCOPE_AGENT); }
#define STAMP(i) do {} while (0)
#define STAMPL(i) do {} while (0)
#define STAMPL_FLUSH(n) do {} while (0)
#define STAMP_BEGIN() do {} while (0)
#define STAMP_END() do {} while (0)

struct Geo { unsigned b, g, j, h, c0; };

#define D_SLOTCHECK(n) do { const unsigned need_ = (n) >= 4 ? (n) - 3 : 0u; if (MODE != 2 && __builtin_expect(rfl(min4(pr)) < need_, 0)) lds_wait4(MISC + L_PROG, need_, true); CBAR(); \
                            STAMPL(ta_); pr = lds_u128(MISC + L_PROG); } while (0)
#define D_PUBLISH(cnt, n) do { STAMPL(tb_); asm volatile("s_waitcnt vmcnt(" #cnt ")" ::: "memory"); lds_st32(fullw, (n) - 1); STAMPL(tc_); STAMPL_FLUSH(n); } while (0)
DI void dma_wave_w(const Geo& G, unsigned dw, const void* kc, const void* wq, const void* wo, unsigned lane, char* ws, unsigned wave) {
  const unsigned fullw = MISC + L_FULL + 4 * dw;
  u64 ta_ = 0, tb_ = 0, tc_ = 0; (void)ta_; (void)tb_; (void)tc_;
  u32x4 pr = {0, 0, 0, 0};
  const __amdgpu_buffer_rsrc_t rA = __builtin_amdgcn_make_buffer_rsrc((void*)wq, 0, DM * NH * DH * 2, 0x00020000);
  unsigned vA[6], rr[3], cc[3];
#pragma unroll
  for (int m = 0; m < 3; ++m) {
    const unsigned u = 64 * m + lane, r = (u * 2731) >> 16, c = u - 24 * r;
    rr[m] = r; cc[m] = c;
    vA[m] = r * 12288 + ((c ^ (r & 2)) << 4);
    vA[m + 3] = (vA[m] ^ 64) + 8 * 12288;
  }
  unsigned kcx = G.c0;
  auto issueA = [&](unsigned n) {
    const unsigned slot = (n & 3) * SLOT + dw * 12288, so = (kcx * 64 + dw * 32) * 12288 + G.h * 384;
#pragma unroll
    for (int p = 0; p < 12; ++p) dma16(rA, slot + p * 1024, vA[p % 6], so + (p / 6) * 16 * 12288);
    kcx = kcx == 9 ? 0u : kcx + 1;
  };
  STAMPL(ta_);
  issueA(0);
  asm volatile("s_barrier" ::: "memory");
  const __amdgpu_buffer_rsrc_t rK = __builtin_amdgcn_make_buffer_rsrc((void*)kc, 0, NB * NH * SK * DH * 2, 0x00020000);
  const __amdgpu_buffer_rsrc_t rB = __builtin_amdgcn_make_buffer_rsrc((void*)wo, 0, DM * NH * DH * 2, 0x00020000);
  unsigned vK[6], vB[5];
#pragma unroll
  for (int m = 0; m < 3; ++m) {
    vK[m] = rr[m] * 384 + ((cc[m] ^ (rr[m] >> 1)) << 4);
    vK[m + 3] = (vK[m] ^ 64) + 8 * 384;
  }
#pragma unroll
  for (int m = 0; m < 5; ++m) {
    const unsigned u = 64 * m + lane, r = (u * 3277) >> 16, c = u - 20 * r;
    vB[m] = r * 1280 + ((c ^ ((r >> 2) & 2)) << 4);
  }
#pragma nounroll
  for (unsigned n = 1; n < 10; ++n) {
    D_SLOTCHECK(n);
    issueA(n);
    D_PUBLISH(24, n);
  }
  { D_SLOTCHECK(10u);
    const unsigned slot = (10 & 3) * SLOT + dw * 12288, so = ((G.b * 32 + G.h) * 64 + dw * 32) * 384;
#pragma unroll
    for (int p = 0; p < 12; ++p) dma16(rK, slot + p * 1024, vK[p % 6], so + (p / 6) * 16 * 384);
    D_PUBLISH(24, 10u); }
  unsigned row = 192 * G.j;
  const unsigned sb = (768 * G.g + dw * 32) * 1280 + 320 * G.j;
#pragma nounroll
  for (unsigned n = 11; n < NCH; ++n) {
    D_SLOTCHECK(n);
    const unsigned slot = (n & 3) * SLOT + dw * 10240, so = sb + row * 1280;
#pragma unroll
    for (int p = 0; p < 10; ++p) dma16(rB, slot + p * 1024, vB[p % 5], so + (p / 5) * 16 * 1280);
    row = row == 704 ? 0u : row + 64;
    D_PUBLISH(20, n);
  }
  asm volatile("s_waitcnt vmcnt(10)" ::: "memory");
  lds_st32(fullw, NCH - 1);
  asm volatile("s_waitcnt vmcnt(0)" ::: "memory");
  lds_st32(fullw, NCH);
  STAMP(3);
}
DI void dma_wave_a(const Geo& G, const void* x, const void* vc, unsigned lane, char* ws, unsigned wave) {
  const unsigned fullw = MISC + L_FULL + 8;
  u64 ta_ = 0, tb_ = 0, tc_ = 0; (void)ta_; (void)tb_; (void)tc_;
  u32x4 pr = {0, 0, 0, 0};
  const __amdgpu_buffer_rsrc_t rA = __builtin_amdgcn_make_buffer_rsrc((void*)x, 0, NB * SQ * DM * 2, 0x00020000);
  const unsigned r = lane >> 3, c = lane & 7, cs = (c ^ (r >> 1)) << 4;
  const unsigned vA0 = r * 1280 + cs, vA1 = r * 1280 + (cs ^ 64);
  unsigned kcx = G.c0;
  auto issueA = [&](unsigned n) {
    const unsigned slot = (n & 3) * SLOT + AUX, so = G.b * 64 * 1280 + kcx * 128;
#pragma unroll
    for (int p = 0; p < 8; ++p) dma16(rA, slot + p * 1024, (p & 1) ? vA1 : vA0, so + p * 8 * 1280);
    kcx = kcx == 9 ? 0u : kcx + 1;
  };
  STAMPL(ta_);
  issueA(0);
  asm volatile("s_barrier" ::: "memory");
  const __amdgpu_buffer_rsrc_t rB = __builtin_amdgcn_make_buffer_rsrc((void*)vc, 0, NB * NH * SK * DH * 2, 0x00020000);
  const unsigned vB0 = r * 384 + cs, vB1 = r * 384 + (cs ^ 64);
#pragma nounroll
  for (unsigned n = 1; n < 10; ++n) {
    D_SLOTCHECK(n);
    issueA(n);
    D_PUBLISH(16, n);
  }
  unsigned hh = G.j, dc = 0;
#pragma nounroll
  for (unsigned n = 11; n < NCH; ++n) {
    D_SLOTCHECK(n);
    const unsigned slot = (n & 3) * SLOT + AUX, so = (G.b * 32 + 4 * G.g + hh) * 24576 + dc * 128;
#pragma unroll
    for (int p = 0; p < 8; ++p) dma16(rB, slot + p * 1024, (p & 1) ? vB1 : vB0, so + p * 8 * 384);
    hh = dc == 2 ? (hh + 1) & 3 : hh;
    dc = dc == 2 ? 0u : dc + 1;
    STAMPL(tb_);
    asm volatile("s_waitcnt vmcnt(16)" ::: "memory");
    lds_st32(fullw, n == 11 ? 9u : n == 12 ? 11u : n - 1);
    STAMPL(tc_); STAMPL_FLUSH(n);
  }
  asm volatile("s_waitcnt vmcnt(8)" ::: "memory");
  lds_st32(fullw, NCH - 1);
  asm volatile("s_waitcnt vmcnt(0)" ::: "memory");
  lds_st32(fullw, NCH);
  STAMP(3);
}

DI void reduce_tile(const Geo& G, unsigned e5, u64 tagR, char* ws, void* out, unsigned lane, bool upd) {
  const unsigned it = G.g >> 1, nt = 5 * (G.g & 1) + e5;
  const unsigned pw = nt < 3 ? 0u : nt < 6 ? 1u : nt < 8 ? 2u : 3u;
  const char* fl = ws + WS_FR + (size_t)((((G.b * 4 + G.j) * 8 + (lane & 7)) * 4 + pw) * 128);
#pragma nounroll
  for (unsigned sp = 0; sp < SPIN_CAP; ++sp) {
    u64 v = ld_sc1_64(fl);
    if (__all(v == tagR)) break;
    __builtin_amdgcn_s_sleep(1);
  }
  CBAR();
  __amdgpu_buffer_rsrc_t rR = __builtin_amdgcn_make_buffer_rsrc((void*)(ws + WS_R), 0, 256 * 40960, 0x00020000);
  const unsigned base = ((G.b * 4 + G.j) * 8) * 40960 + (it * 10 + nt) * 1024;
  u32x4 v[8];
#pragma unroll
  for (int s = 0; s < 8; ++s) v[s] = __builtin_amdgcn_raw_buffer_load_b128(rR, lane * 16, base + s * 40960, LD_AUX);
  f32x4 a = __builtin_bit_cast(f32x4, v[0]);
#pragma unroll
  for (int s = 1; s < 8; ++s) a += __builtin_bit_cast(f32x4, v[s]);
  u32x2 o = {cvtpk(a[0], a[1]), cvtpk(a[2], a[3])};
  __amdgpu_buffer_rsrc_t rO = __builtin_amdgcn_make_buffer_rsrc(out, 0, NB * SQ * DM * 2, 0x00020000);
  const unsigned row = G.b * 64 + 16 * it + (lane & 15), col = 160 * G.j + 16 * nt + 4 * (lane >> 4);
  __builtin_amdgcn_raw_buffer_store_b64(o, rO, (row * DM + col) * 2, 0, 0);
  if (upd && lane == 0) st_sc1_64(ws + WS_ER + (G.b * 4 + G.j) * 128, tagR);
}

struct Frag { bf16x8 w[3]; bf16x8 a[4]; };
template <unsigned WROW, bool AMAJOR>
DI void load_unit(Frag& F, const unsigned (&wb)[3], unsigned ab, int ks) {
  if (AMAJOR) {
    F.a[0] = lds_b128(ab);
#pragma unroll
    for (int e = 0; e < 3; ++e) { const unsigned a = wb[e] + ks * 32 * WROW; F.w[e] = lds_tr(a, a + 4 * WROW); }
#pragma unroll
    for (int t = 1; t < 4; ++t) F.a[t] = lds_b128(ab + t * 2048);
  } else {
    { const unsigned a = wb[0] + ks * 32 * WROW; F.w[0] = lds_tr(a, a + 4 * WROW); }
#pragma unroll
    for (int t = 0; t < 4; ++t) F.a[t] = lds_b128(ab + t * 2048);
#pragma unroll
    for (int e = 1; e < 3; ++e) { const unsigned a = wb[e] + ks * 32 * WROW; F.w[e] = lds_tr(a, a + 4 * WROW); }
  }
}
DI void interleave() {
#pragma unroll
  for (int i = 0; i < 10; ++i) { __builtin_amdgcn_sched_group_barrier(0x100, 1, 0); __builtin_amdgcn_sched_group_barrier(0x8, 1, 0); }
  __builtin_amdgcn_sched_group_barrier(0x8, 2, 0);
}

__global__ void __launch_bounds__(512, 2)
mha_fused(const void* __restrict__ x, const void* __restrict__ kc, const void* __restrict__ vc, const void* __restrict__ wq,
          const void* __restrict__ wo, void* __restrict__ out, char* __restrict__ ws) {
  const unsigned tid = threadIdx.x, lane = tid & 63, wave = rfl(tid >> 6);
  Geo G;
  { const unsigned bid = blockIdx.x; G.g = bid & 7; const unsigned idx = bid >> 3; G.j = idx & 3; G.b = idx >> 2; G.h = 4 * G.g + G.j;
    const unsigned fb = (G.b * 10) >> 3, oj = (G.j * 10 + 2) >> 2;
    unsigned c0 = fb + oj; if (c0 >= 10) c0 -= 10; G.c0 = ROTATE ? c0 : 0u; }
  STAMP_BEGIN();
  if (wave >= 4 && wave < 7) {
    if (MODE == 1) { asm volatile("s_barrier" ::: "memory"); }
    else if (wave < 6) dma_wave_w(G, wave - 4, kc, wq, wo, lane, ws, wave);
    else dma_wave_a(G, x, vc, lane, ws, wave);
    if (MODE != 0) { STAMP_END(); return; }
    lds_wait1(MISC + L_PFULL, 3, true);
    const u64 tagR = (u64)lds_u32(MISC + L_TAG) | ((u64)lds_u32(MISC + L_TAG + 4) << 32);
#pragma nounroll
    for (unsigned e5 = wave - 4; e5 < 5; e5 += 4) reduce_tile(G, e5, tagR, ws, out, lane, e5 == 0 && G.g == 0);
    STAMP(5);
  } else if (wave == 7) {
    const u64 eP = ld_sc1_64(ws + WS_EP + (G.b * 8 + G.g) * 128);
    const u64 eR = ld_sc1_64(ws + WS_ER + (G.b * 4 + G.j) * 128);
    asm volatile("s_barrier" ::: "memory");
    if (MODE != 0) { STAMP_END(); return; }
    const u64 tagP = next_tag(eP), tagR = next_tag(eR);
    if (lane == 0) { lds_st32(MISC + L_TAG, (unsigned)tagR); lds_st32(MISC + L_TAG + 4, (unsigned)(tagR >> 32)); }
    STAMP(1);
    lds_wait4(MISC + L_POWN, 1, true);
    STAMP(2);
    __amdgpu_buffer_rsrc_t rP = __builtin_amdgcn_make_buffer_rsrc((void*)(ws + WS_P), 0, 256 * 8192, 0x00020000);
    const unsigned pl = opaque(PBUF + lane * 16);
    {
      u32x4 f[8];
#pragma unroll
      for (int i = 0; i < 8; ++i) f[i] = *(const LAS u32x4*)(uintptr_t)(pl + (i >> 1) * 6144 + (i & 1) * 1024);
#pragma unroll
      for (int i = 0; i < 8; ++i) __builtin_amdgcn_raw_buffer_store_b128(f[i], rP, lane * 16, (G.b * 32 + G.h) * 8192 + i * 1024, ST_AUX);
      asm volatile("s_waitcnt vmcnt(0)" ::: "memory");
      if (lane == 0) st_sc1_64(ws + WS_FP + (G.b * 32 + G.h) * 128, tagP);
    }
    STAMP(3);
    { const char* fl = ws + WS_FP + (size_t)(G.b * 32 + 4 * G.g + (lane & 3)) * 128;
#pragma nounroll
      for (unsigned sp = 0; sp < SPIN_CAP; ++sp) {
        u64 v = ld_sc1_64(fl);
        if (__all(v == tagP)) break;
        __builtin_amdgcn_s_sleep(1);
      }
      CBAR(); }
    STAMP(4);
    if (G.j == 0 && lane == 0) st_sc1_64(ws + WS_EP + (G.b * 8 + G.g) * 128, tagP);
    {
      u32x4 f[3][8];
#pragma unroll
      for (int u = 0; u < 3; ++u) {
        const unsigned hh = (G.j + u + 1) & 3;
#pragma unroll
        for (int i = 0; i < 8; ++i) f[u][i] = __builtin_amdgcn_raw_buffer_load_b128(rP, lane * 16, (G.b * 32 + 4 * G.g + hh) * 8192 + i * 1024, LD_AUX);
      }
#pragma unroll
      for (int u = 0; u < 2; ++u)
#pragma unroll
        for (int i = 0; i < 8; ++i) *(LAS u32x4*)(uintptr_t)(pl + (i >> 1) * 6144 + (u + 1) * 2048 + (i & 1) * 1024) = f[u][i];
      CBAR();
      lds_st32(MISC + L_PFULL, 3);
      lds_wait4(MISC + L_PVW, 1, true);
#pragma unroll
      for (int i = 0; i < 8; ++i) *(LAS u32x4*)(uintptr_t)(pl + (i >> 1) * 6144 + (i & 1) * 1024) = f[2][i];
      CBAR();
      lds_st32(MISC + L_PFULL, 4);
    }
    STAMP(5);
    reduce_tile(G, 3, tagR, ws, out, lane, false);
    STAMP(6);
  } else {
    if (tid < 32) lds_st32(MISC + 4 * tid, 0);
    asm volatile("s_waitcnt lgkmcnt(0)\n\ts_barrier" ::: "memory");
    if (MODE == 2) { STAMP_END(); return; }
    const unsigned i16 = lane & 15, g4 = lane >> 4, q = i16 >> 2, p = lane & 3;
    const unsigned a0 = AUX + i16 * 128 + ((g4 ^ (i16 >> 1)) << 4), a1 = a0 ^ 64;
    Frag F0, F1;
    f32x4 acc[3][4];
#pragma unroll
    for (int e = 0; e < 3; ++e)
#pragma unroll
      for (int t = 0; t < 4; ++t) acc[e][t] = (f32x4){0.f, 0.f, 0.f, 0.f};
    {
      unsigned wa[3];
      const unsigned phi = ((q >> 1) & 1) | ((g4 & 1) << 1);
#pragma unroll
      for (int e = 0; e < 3; ++e) wa[e] = (8 * g4 + q) * 384 + 32 * ((3 * wave + e) ^ phi) + 16 * (p >> 1) + 8 * (p & 1);
      unsigned wb[3], ab0, ab1;
      lds_wait3(MISC + L_FULL, 1);
      STAMP(1);
#pragma unroll
      for (int e = 0; e < 3; ++e) wb[e] = opaque(wa[e]);
      ab0 = opaque(a0); ab1 = opaque(a1);
      load_unit<384, false>(F0, wb, ab0, 0);
#pragma nounroll
      for (unsigned c = 0; c < 10; ++c) {
        SB0();
        const u32x4 fl = lds_u128(MISC + L_FULL);
        load_unit<384, false>(F1, wb, ab1, 1);
        lds_st32(MISC + L_PROG + 4 * wave, c + 1);
#pragma unroll
        for (int e = 0; e < 3; ++e)
#pragma unroll
          for (int t = 0; t < 4; ++t) acc[e][t] = MFMA(F0.w[e], F0.a[t], acc[e][t]);
        interleave();
        SB0();
        {
          const unsigned need = c < 9 ? c + 2 : 0u;
          if (MODE != 1 && rfl(min3v(fl)) < need) lds_wait3(MISC + L_FULL, need);
          CBAR();
          const unsigned slot = ((c + 1) & 3) * SLOT;
#pragma unroll
          for (int e = 0; e < 3; ++e) wb[e] = opaque(slot + wa[e]);
          ab0 = opaque(slot + a0); ab1 = opaque(slot + a1);
          SB0();
          load_unit<384, false>(F0, wb, ab0, 0);
        }
#pragma unroll
        for (int e = 0; e < 3; ++e)
#pragma unroll
          for (int t = 0; t < 4; ++t) acc[e][t] = MFMA(F1.w[e], F1.a[t], acc[e][t]);
        interleave();
        SB0();
      }
    }
    STAMP(2);
    {
      const unsigned qw = opaque(PBUF + ((g4 >> 1) * 16 + i16) * 16 + (g4 & 1) * 8);
#pragma unroll
      for (int e = 0; e < 3; ++e) {
        const unsigned tau = 3 * wave + e;
        const unsigned qe = qw + (tau >> 1) * 1024 + (tau & 1) * 512;
#pragma unroll
        for (int t = 0; t < 4; ++t) { u32x2 v = {cvtpk(acc[e][t][0], acc[e][t][1]), cvtpk(acc[e][t][2], acc[e][t][3])};
          *(LAS u32x2*)(uintptr_t)(qe + t * 6144) = v; }
      }
      CBAR();
      lds_st32(MISC + L_QD + 4 * wave, 1);
    }
    lds_wait3(MISC + L_FULL, 11);
    lds_wait4(MISC + L_QD, 1, false);
    STAMP(3);
    f32x4 st[4];
    {
      const unsigned qr = opaque(PBUF + wave * 6144 + lane * 16);
      bf16x8 qf[6];
#pragma unroll
      for (int s = 0; s < 6; ++s) qf[s] = lds_b128(qr + s * 1024);
      const unsigned slot = (10 & 3) * SLOT, fk = i16 >> 1;
      unsigned ka[2];
#pragma unroll
      for (int so = 0; so < 2; ++so) ka[so] = opaque(slot + i16 * 384 + (((4 * so + g4) ^ fk) << 4));
      bf16x8 kf[6][4];
#pragma unroll
      for (int s = 0; s < 6; ++s)
#pragma unroll
        for (int kt = 0; kt < 4; ++kt) kf[s][kt] = lds_b128(ka[s & 1] + kt * 16 * 384 + 128 * (s >> 1));
      lds_st32(MISC + L_PROG + 4 * wave, 11);
      SB0();
#pragma unroll
      for (int kt = 0; kt < 4; ++kt) st[kt] = (f32x4){0.f, 0.f, 0.f, 0.f};
#pragma unroll
      for (int s = 0; s < 6; ++s)
#pragma unroll
        for (int kt = 0; kt < 4; ++kt) st[kt] = MFMA(kf[s][kt], qf[s], st[kt]);
    }
    {
      float m = fmaxf(fmaxf(st[0][0], st[0][1]), fmaxf(st[0][2], st[0][3]));
#pragma unroll
      for (int kt = 1; kt < 4; ++kt) m = fmaxf(fmaxf(m, fmaxf(st[kt][0], st[kt][1])), fmaxf(st[kt][2], st[kt][3]));
      { auto r1 = __builtin_amdgcn_permlane16_swap(__float_as_uint(m), __float_as_uint(m), false, false);
        m = fmaxf(__uint_as_float(r1[0]), __uint_as_float(r1[1]));
        auto r2 = __builtin_amdgcn_permlane32_swap(__float_as_uint(m), __float_as_uint(m), false, false);
        m = fmaxf(__uint_as_float(r2[0]), __uint_as_float(r2[1])); }
      constexpr float SC = 0.07216878364870322f * 1.4426950408889634f;
      const float ml = m * SC;
      float sum = 0.f;
#pragma unroll
      for (int kt = 0; kt < 4; ++kt)
#pragma unroll
        for (int r = 0; r < 4; ++r) { const float e = __builtin_amdgcn_exp2f(__builtin_fmaf(st[kt][r], SC, -ml)); st[kt][r] = e; sum += e; }
      { auto r1 = __builtin_amdgcn_permlane16_swap(__float_as_uint(sum), __float_as_uint(sum), false, false);
        sum = __uint_as_float(r1[0]) + __uint_as_float(r1[1]);
        auto r2 = __builtin_amdgcn_permlane32_swap(__float_as_uint(sum), __float_as_uint(sum), false, false);
        sum = __uint_as_float(r2[0]) + __uint_as_float(r2[1]); }
      const float inv = __builtin_amdgcn_rcpf(sum);
#pragma unroll
      for (int kt = 0; kt < 4; ++kt) st[kt] *= inv;
      const unsigned pw = opaque(PBUF + wave * 6144 + lane * 16);
#pragma unroll
      for (int s2 = 0; s2 < 2; ++s2) *(LAS bf16x8*)(uintptr_t)(pw + s2 * 1024) = pack8(st[2 * s2], st[2 * s2 + 1]);
      CBAR();
      lds_st32(MISC + L_POWN + 4 * wave, 1);
    }
    STAMP(4);
    const unsigned ntb = wave == 3 ? 7u : 3u * wave;
    unsigned ba[3];
#pragma unroll
    for (int e = 0; e < 3; ++e) ba[e] = (8 * g4 + q) * 320 + 32 * ((ntb + e) ^ (g4 & 1)) + 16 * (p >> 1) + 8 * (p & 1);
    f32x4 oa[4][3];
#pragma unroll
    for (int it = 0; it < 4; ++it)
#pragma unroll
      for (int e = 0; e < 3; ++e) oa[it][e] = (f32x4){0.f, 0.f, 0.f, 0.f};
    auto pvw = [&](unsigned buf, const bf16x8 (&pk)[2][3]) {
      const unsigned pb = opaque(PBUF + buf * 2048 + lane * 16);
      bf16x8 pf[4][2];
#pragma unroll
      for (int it = 0; it < 4; ++it)
#pragma unroll
        for (int s2 = 0; s2 < 2; ++s2) pf[it][s2] = lds_b128(pb + it * 6144 + s2 * 1024);
      SB0();
#pragma unroll
      for (int s2 = 0; s2 < 2; ++s2)
#pragma unroll
        for (int it = 0; it < 4; ++it)
#pragma unroll
          for (int e = 0; e < 3; ++e) oa[it][e] = MFMA(pk[s2][e], pf[it][s2], oa[it][e]);
      SB0();
    };
    bf16x8 pkA[2][3];
#pragma unroll
    for (int s2 = 0; s2 < 2; ++s2)
#pragma unroll
      for (int e = 0; e < 3; ++e) pkA[s2][e] = F0.a[0];
    f32x4 vw[4][3];
#pragma unroll
    for (int kt = 0; kt < 4; ++kt)
#pragma unroll
      for (int e = 0; e < 3; ++e) vw[kt][e] = (f32x4){0.f, 0.f, 0.f, 0.f};
    {
      unsigned wb[3], ab0, ab1;
      lds_wait3(MISC + L_FULL, 12);
      STAMP(11);
      { const unsigned slot = (11 & 3) * SLOT;
#pragma unroll
        for (int e = 0; e < 3; ++e) wb[e] = opaque(slot + ba[e]);
        ab0 = opaque(slot + a0); ab1 = opaque(slot + a1); }
      load_unit<320, true>(F0, wb, ab0, 0);
      unsigned dc = 0, u = 0;
#pragma nounroll
      for (unsigned c = 11; c < NCH; ++c) {
        SB0();
        const u32x4 fl = lds_u128(MISC + L_FULL);
        load_unit<320, true>(F1, wb, ab1, 1);
        lds_st32(MISC + L_PROG + 4 * wave, c + 1);
#pragma unroll
        for (int kt = 0; kt < 4; ++kt)
#pragma unroll
          for (int e = 0; e < 3; ++e) vw[kt][e] = MFMA(F0.a[kt], F0.w[e], vw[kt][e]);
        interleave();
        SB0();
        {
          const unsigned need = c < NCH - 1 ? c + 2 : 0u;
          if (MODE != 1 && rfl(min3v(fl)) < need) lds_wait3(MISC + L_FULL, need);
          CBAR();
          const unsigned slot = ((c + 1) & 3) * SLOT;
#pragma unroll
          for (int e = 0; e < 3; ++e) wb[e] = opaque(slot + ba[e]);
          ab0 = opaque(slot + a0); ab1 = opaque(slot + a1);
          SB0();
          load_unit<320, true>(F0, wb, ab0, 0);
        }
#pragma unroll
        for (int kt = 0; kt < 4; ++kt)
#pragma unroll
          for (int e = 0; e < 3; ++e) vw[kt][e] = MFMA(F1.a[kt], F1.w[e], vw[kt][e]);
        interleave();
        SB0();
        if (++dc == 3) {
          dc = 0;
          bf16x8 pk[2][3];
#pragma unroll
          for (int s2 = 0; s2 < 2; ++s2)
#pragma unroll
            for (int e = 0; e < 3; ++e) pk[s2][e] = pack8(vw[2 * s2][e], vw[2 * s2 + 1][e]);
#pragma unroll
          for (int kt = 0; kt < 4; ++kt)
#pragma unroll
            for (int e = 0; e < 3; ++e) vw[kt][e] = (f32x4){0.f, 0.f, 0.f, 0.f};
          STAMP(5 + u);
          if (u == 1) {
#pragma unroll
            for (int s2 = 0; s2 < 2; ++s2)
#pragma unroll
              for (int e = 0; e < 3; ++e) pkA[s2][e] = pk[s2][e];
          } else {
            if (u == 0) lds_wait4(MISC + L_POWN, 1, false);
            else if (MODE == 0) lds_wait1(MISC + L_PFULL, u + 1, false);
            if (u == 2) pvw(1, pkA);
            pvw(u == 2 ? 2u : 0u, pk);
            if (u == 0) { CBAR(); lds_st32(MISC + L_PVW + 4 * wave, 1); }
          }
          ++u;
        }
      }
    }
    STAMP(9);
    if (MODE != 0) { if (oa[0][0][0] == 1.2345f && oa[3][2][1] == 7.f && oa[1][1][2] == oa[2][0][0] && oa[0][1][1] == oa[1][2][3] && oa[2][1][1] == oa[3][0][0] && oa[0][2][0] == oa[1][0][0] && oa[2][2][0] == oa[3][1][0]) lds_st32(0, 1); STAMP_END(); return; }
    const u64 tagR = (u64)lds_u32(MISC + L_TAG) | ((u64)lds_u32(MISC + L_TAG + 4) << 32);
    __amdgpu_buffer_rsrc_t rR = __builtin_amdgcn_make_buffer_rsrc((void*)(ws + WS_R), 0, 256 * 40960, 0x00020000);
    const unsigned base = ((G.b * 4 + G.j) * 8 + G.g) * 40960;
#pragma unroll
    for (int e = 0; e < 3; ++e) {
      const bool keep = wave < 2 || (wave == 2 ? e < 2 : e > 0);
      if (keep) {
#pragma unroll
        for (int it = 0; it < 4; ++it)
          __builtin_amdgcn_raw_buffer_store_b128(__builtin_bit_cast(u32x4, oa[it][e]), rR, lane * 16, base + (it * 10 + ntb + e) * 1024, ST_AUX);
      }
    }
    asm volatile("s_waitcnt vmcnt(0)" ::: "memory");
    if (lane == 0) st_sc1_64(ws + WS_FR + (size_t)((((G.b * 4 + G.j) * 8 + G.g) * 4 + wave) * 128), tagR);
    STAMP(10);
  }
  STAMP_END();
}

extern "C" void kernel_launch(void* const* d_in, const int* in_sizes, int n_in, void* d_out, int out_size, void* d_ws, size_t ws_size,
                              hipStream_t stream) {
  (void)in_sizes; (void)n_in; (void)out_size; (void)ws_size;
  (void)hipFuncSetAttribute((const void*)mha_fused, hipFuncAttributeMaxDynamicSharedMemorySize, LDS_BYTES);
  mha_fused<<<256, 512, LDS_BYTES, stream>>>(d_in[0], d_in[1], d_in[2], d_in[3], d_in[4], d_out, (char*)d_ws);
}
```

```cpp
#include <hip/hip_runtime.h>
#include <hip/hip_bf16.h>
#include <stdint.h>

#define LAS __attribute__((address_space(3)))
#define GAS __attribute__((address_space(1)))
#define DI __device__ __forceinline__
typedef short s16x4 __attribute__((ext_vector_type(4)));
typedef __bf16 bf16x2 __attribute__((ext_vector_type(2)));
typedef __bf16 bf16x8 __attribute__((ext_vector_type(8)));
typedef float f32x2 __attribute__((ext_vector_type(2)));
typedef float f32x4 __attribute__((ext_vector_type(4)));
typedef unsigned u32x2 __attribute__((ext_vector_type(2)));
typedef unsigned u32x4 __attribute__((ext_vector_type(4)));
typedef unsigned long long u64;

constexpr int NB = 8, SQ = 64, DM = 640, NH = 32, SK = 64, DH = 192;
constexpr int NCH = 23;
constexpr unsigned SLOT = 32768, AUX = 24576;
constexpr unsigned PBUF = 131072, MISC = PBUF + 3 * 8192, LDS_USED = MISC + 256;
constexpr unsigned L_TAGP = 0, L_TAGR = 8;
constexpr unsigned WS_EP = 0;
constexpr unsigned WS_ER = 8192;
constexpr unsigned WS_FP = 16384;
constexpr unsigned WS_FR = WS_FP + 131072;
constexpr unsigned WS_P = 524288;
constexpr unsigned WS_R = WS_P + 256 * 8192;
constexpr unsigned WS_DBG = 16u << 20;
constexpr unsigned SPIN_CAP = 1u << 16;

#define STAMPS 0
#define ROTATE 0
#define IN_AUX 0
#define ST_AUX 16
#define LD_AUX 16
constexpr unsigned LDS_BYTES = LDS_USED + (STAMPS ? 4096 : 0);

DI unsigned rfl(unsigned x) { return __builtin_amdgcn_readfirstlane(x); }
DI unsigned cvtpk(float lo, float hi) { f32x2 v = {lo, hi}; bf16x2 b = __builtin_convertvector(v, bf16x2); return __builtin_bit_cast(unsigned, b); }
DI bf16x8 pack8(const f32x4& a, const f32x4& b) {
  u32x4 p = {cvtpk(a[0], a[1]), cvtpk(a[2], a[3]), cvtpk(b[0], b[1]), cvtpk(b[2], b[3])};
  return __builtin_bit_cast(bf16x8, p);
}
#define MFMA(a, b, c) __builtin_amdgcn_mfma_f32_16x16x32_bf16((a), (b), (c), 0, 0, 0)
DI bf16x8 lds_b128(unsigned addr) { return *(const LAS bf16x8*)(uintptr_t)addr; }
DI unsigned lds_u32(unsigned addr) { return *(const volatile LAS unsigned*)(uintptr_t)addr; }
DI void lds_st32(unsigned addr, unsigned v) { *(volatile LAS unsigned*)(uintptr_t)addr = v; }
DI u64 lds_u64(unsigned addr) { return (u64)lds_u32(addr) | ((u64)lds_u32(addr + 4) << 32); }
#define CBAR() asm volatile("" ::: "memory")
#define SB0() __builtin_amdgcn_sched_barrier(0)
#define BAR() asm volatile("s_barrier" ::: "memory")
#define BAR_L() asm volatile("s_waitcnt lgkmcnt(0)\n\ts_barrier" ::: "memory")
#define WAITVM(n) asm volatile("s_waitcnt vmcnt(" #n ")" ::: "memory")
DI void dma16(__amdgpu_buffer_rsrc_t r, unsigned lds, unsigned voff, unsigned soff) {
  __builtin_amdgcn_raw_ptr_buffer_load_lds(r, (LAS void*)(uintptr_t)lds, 16, voff, soff, 0, IN_AUX);
}
DI unsigned opaque(unsigned x) { asm volatile("" : "+v"(x)); return x; }
DI float max3f(float a, float b, float c) { float r; asm("v_max3_f32 %0, %1, %2, %3" : "=v"(r) : "v"(a), "v"(b), "v"(c)); return r; }
DI u64 next_tag(u64 e) { return e * 6364136223846793005ull + 1442695040888963407ull; }
DI u64 ld_sc1_64(const void* p) { return __hip_atomic_load((const GAS u64*)p, __ATOMIC_RELAXED, __HIP_MEMORY_SCOPE_AGENT); }
DI void st_sc1_64(void* p, u64 v) { __hip_atomic_store((GAS u64*)p, v, __ATOMIC_RELAXED, __HIP_MEMORY_SCOPE_AGENT); }
DI bool vm_idle() { unsigned r; asm volatile("s_getreg_b32 %0, hwreg(HW_REG_IB_STS)" : "=s"(r) :: "memory"); return (r & 0x00C0000Fu) == 0; }
DI bool vm_idle_soon() {
#pragma nounroll
  for (int k = 0; k < 4; ++k) { if (vm_idle()) return true; __builtin_amdgcn_s_sleep(1); }
  return false;
}
#define STAMP(i) do {} while (0)
#define STAMP_BEGIN() do {} while (0)
#define STAMP_END() do {} while (0)
#define STAMPD(i) do {} while (0)

#define ASM_CHUNK_A \
  "s_nop 1\n\t" \
  "s_waitcnt lgkmcnt(0)\n\t" \
  "v_mfma_f32_16x16x32_bf16 %0, v[200:203], v[212:215], %0\n\t" \
  "ds_read_b64_tr_b16 v[228:229], %19 offset:12288\n\t" \
  "ds_read_b64_tr_b16 v[230:231], %19 offset:13824\n\t" \
  "v_mfma_f32_16x16x32_bf16 %1, v[200:203], v[216:219], %1\n\t" \
  "ds_read_b128 v[240:243], %23 offset:0\n\t" \
  "ds_read_b128 v[244:247], %23 offset:2048\n\t" \
  "v_mfma_f32_16x16x32_bf16 %2, v[200:203], v[220:223], %2\n\t" \
  "ds_read_b128 v[248:251], %23 offset:4096\n\t" \
  "ds_read_b128 v[252:255], %23 offset:6144\n\t" \
  "v_mfma_f32_16x16x32_bf16 %3, v[200:203], v[224:227], %3\n\t" \
  "ds_read_b64_tr_b16 v[232:233], %20 offset:12288\n\t" \
  "ds_read_b64_tr_b16 v[234:235], %20 offset:13824\n\t" \
  "v_mfma_f32_16x16x32_bf16 %4, v[204:207], v[212:215], %4\n\t" \
  "ds_read_b64_tr_b16 v[236:237], %21 offset:12288\n\t" \
  "ds_read_b64_tr_b16 v[238:239], %21 offset:13824\n\t" \
  "v_mfma_f32_16x16x32_bf16 %5, v[204:207], v[216:219], %5\n\t" \
  "v_add_u32 %19, %24, %19\n\t" \
  "v_mfma_f32_16x16x32_bf16 %6, v[204:207], v[220:223], %6\n\t" \
  "v_add_u32 %20, %24, %20\n\t" \
  "v_mfma_f32_16x16x32_bf16 %7, v[204:207], v[224:227], %7\n\t" \
  "v_add_u32 %21, %24, %21\n\t" \
  "v_mfma_f32_16x16x32_bf16 %8, v[208:211], v[212:215], %8\n\t" \
  "v_add_u32 %22, %24, %22\n\t" \
  "v_mfma_f32_16x16x32_bf16 %9, v[208:211], v[216:219], %9\n\t" \
  "v_add_u32 %23, %24, %23\n\t" \
  "v_mfma_f32_16x16x32_bf16 %10, v[208:211], v[220:223], %10\n\t" \
  "v_mfma_f32_16x16x32_bf16 %11, v[208:211], v[224:227], %11\n\t" \
  "s_waitcnt lgkmcnt(0)\n\t" \
  "s_barrier\n\t" \
  "v_mfma_f32_16x16x32_bf16 %0, v[228:231], v[240:243], %0\n\t" \
  "ds_read_b64_tr_b16 v[200:201], %19 offset:0\n\t" \
  "ds_read_b64_tr_b16 v[202:203], %19 offset:1536\n\t" \
  "v_mfma_f32_16x16x32_bf16 %1, v[228:231], v[244:247], %1\n\t" \
  "ds_read_b128 v[212:215], %22 offset:0\n\t" \
  "ds_read_b128 v[216:219], %22 offset:2048\n\t" \
  "v_mfma_f32_16x16x32_bf16 %2, v[228:231], v[248:251], %2\n\t" \
  "ds_read_b128 v[220:223], %22 offset:4096\n\t" \
  "ds_read_b128 v[224:227], %22 offset:6144\n\t" \
  "v_mfma_f32_16x16x32_bf16 %3, v[228:231], v[252:255], %3\n\t" \
  "ds_read_b64_tr_b16 v[204:205], %20 offset:0\n\t" \
  "ds_read_b64_tr_b16 v[206:207], %20 offset:1536\n\t" \
  "v_mfma_f32_16x16x32_bf16 %4, v[232:235], v[240:243], %4\n\t" \
  "ds_read_b64_tr_b16 v[208:209], %21 offset:0\n\t" \
  "ds_read_b64_tr_b16 v[210:211], %21 offset:1536\n\t" \
  "v_mfma_f32_16x16x32_bf16 %5, v[232:235], v[244:247], %5\n\t" \
  "v_mfma_f32_16x16x32_bf16 %6, v[232:235], v[248:251], %6\n\t" \
  "v_mfma_f32_16x16x32_bf16 %7, v[232:235], v[252:255], %7\n\t" \
  "v_mfma_f32_16x16x32_bf16 %8, v[236:239], v[240:243], %8\n\t" \
  "v_mfma_f32_16x16x32_bf16 %9, v[236:239], v[244:247], %9\n\t" \
  "v_mfma_f32_16x16x32_bf16 %10, v[236:239], v[248:251], %10\n\t" \
  "v_mfma_f32_16x16x32_bf16 %11, v[236:239], v[252:255], %11\n\t" \
  "s_waitcnt lgkmcnt(0)\n\t" \
  "s_nop 7\n\t"

#define ASM_CHUNK_B \
  "s_nop 1\n\t" \
  "s_waitcnt lgkmcnt(0)\n\t" \
  "v_mfma_f32_16x16x32_bf16 %0, v[212:215], v[200:203], %0\n\t" \
  "ds_read_b128 v[240:243], %23 offset:0\n\t" \
  "ds_read_b64_tr_b16 v[228:229], %19 offset:10240\n\t" \
  "v_mfma_f32_16x16x32_bf16 %1, v[212:215], v[204:207], %1\n\t" \
  "ds_read_b64_tr_b16 v[230:231], %19 offset:11520\n\t" \
  "ds_read_b64_tr_b16 v[232:233], %20 offset:10240\n\t" \
  "v_mfma_f32_16x16x32_bf16 %2, v[212:215], v[208:211], %2\n\t" \
  "ds_read_b64_tr_b16 v[234:235], %20 offset:11520\n\t" \
  "ds_read_b64_tr_b16 v[236:237], %21 offset:10240\n\t" \
  "v_mfma_f32_16x16x32_bf16 %3, v[216:219], v[200:203], %3\n\t" \
  "ds_read_b64_tr_b16 v[238:239], %21 offset:11520\n\t" \
  "ds_read_b128 v[244:247], %23 offset:2048\n\t" \
  "v_mfma_f32_16x16x32_bf16 %4, v[216:219], v[204:207], %4\n\t" \
  "ds_read_b128 v[248:251], %23 offset:4096\n\t" \
  "ds_read_b128 v[252:255], %23 offset:6144\n\t" \
  "v_mfma_f32_16x16x32_bf16 %5, v[216:219], v[208:211], %5\n\t" \
  "v_add_u32 %19, %24, %19\n\t" \
  "v_mfma_f32_16x16x32_bf16 %6, v[220:223], v[200:203], %6\n\t" \
  "v_add_u32 %20, %24, %20\n\t" \
  "v_mfma_f32_16x16x32_bf16 %7, v[220:223], v[204:207], %7\n\t" \
  "v_add_u32 %21, %24, %21\n\t" \
  "v_mfma_f32_16x16x32_bf16 %8, v[220:223], v[208:211], %8\n\t" \
  "v_add_u32 %22, %24, %22\n\t" \
  "v_mfma_f32_16x16x32_bf16 %9, v[224:227], v[200:203], %9\n\t" \
  "v_add_u32 %23, %24, %23\n\t" \
  "v_mfma_f32_16x16x32_bf16 %10, v[224:227], v[204:207], %10\n\t" \
  "v_mfma_f32_16x16x32_bf16 %11, v[224:227], v[208:211], %11\n\t" \
  "s_waitcnt lgkmcnt(0)\n\t" \
  "s_barrier\n\t" \
  "v_mfma_f32_16x16x32_bf16 %0, v[240:243], v[228:231], %0\n\t" \
  "ds_read_b128 v[212:215], %22 offset:0\n\t" \
  "ds_read_b64_tr_b16 v[200:201], %19 offset:0\n\t" \
  "v_mfma_f32_16x16x32_bf16 %1, v[240:243], v[232:235], %1\n\t" \
  "ds_read_b64_tr_b16 v[202:203], %19 offset:1280\n\t" \
  "ds_read_b64_tr_b16 v[204:205], %20 offset:0\n\t" \
  "v_mfma_f32_16x16x32_bf16 %2, v[240:243], v[236:239], %2\n\t" \
  "ds_read_b64_tr_b16 v[206:207], %20 offset:1280\n\t" \
  "ds_read_b64_tr_b16 v[208:209], %21 offset:0\n\t" \
  "v_mfma_f32_16x16x32_bf16 %3, v[244:247], v[228:231], %3\n\t" \
  "ds_read_b64_tr_b16 v[210:211], %21 offset:1280\n\t" \
  "ds_read_b128 v[216:219], %22 offset:2048\n\t" \
  "v_mfma_f32_16x16x32_bf16 %4, v[244:247], v[232:235], %4\n\t" \
  "ds_read_b128 v[220:223], %22 offset:4096\n\t" \
  "ds_read_b128 v[224:227], %22 offset:6144\n\t" \
  "v_mfma_f32_16x16x32_bf16 %5, v[244:247], v[236:239], %5\n\t" \
  "v_mfma_f32_16x16x32_bf16 %6, v[248:251], v[228:231], %6\n\t" \
  "v_mfma_f32_16x16x32_bf16 %7, v[248:251], v[232:235], %7\n\t" \
  "v_mfma_f32_16x16x32_bf16 %8, v[248:251], v[236:239], %8\n\t" \
  "v_mfma_f32_16x16x32_bf16 %9, v[252:255], v[228:231], %9\n\t" \
  "v_mfma_f32_16x16x32_bf16 %10, v[252:255], v[232:235], %10\n\t" \
  "v_mfma_f32_16x16x32_bf16 %11, v[252:255], v[236:239], %11\n\t" \
  "s_waitcnt lgkmcnt(0)\n\t" \
  "s_nop 7\n\t"

#define ASM_CHUNK_B0 \
  "s_nop 1\n\t" \
  "s_waitcnt lgkmcnt(0)\n\t" \
  "v_mfma_f32_16x16x32_bf16 %0, v[212:215], v[200:203], 0\n\t" \
  "ds_read_b128 v[240:243], %23 offset:0\n\t" \
  "ds_read_b64_tr_b16 v[228:229], %19 offset:10240\n\t" \
  "v_mfma_f32_16x16x32_bf16 %1, v[212:215], v[204:207], 0\n\t" \
  "ds_read_b64_tr_b16 v[230:231], %19 offset:11520\n\t" \
  "ds_read_b64_tr_b16 v[232:233], %20 offset:10240\n\t" \
  "v_mfma_f32_16x16x32_bf16 %2, v[212:215], v[208:211], 0\n\t" \
  "ds_read_b64_tr_b16 v[234:235], %20 offset:11520\n\t" \
  "ds_read_b64_tr_b16 v[236:237], %21 offset:10240\n\t" \
  "v_mfma_f32_16x16x32_bf16 %3, v[216:219], v[200:203], 0\n\t" \
  "ds_read_b64_tr_b16 v[238:239], %21 offset:11520\n\t" \
  "ds_read_b128 v[244:247], %23 offset:2048\n\t" \
  "v_mfma_f32_16x16x32_bf16 %4, v[216:219], v[204:207], 0\n\t" \
  "ds_read_b128 v[248:251], %23 offset:4096\n\t" \
  "ds_read_b128 v[252:255], %23 offset:6144\n\t" \
  "v_mfma_f32_16x16x32_bf16 %5, v[216:219], v[208:211], 0\n\t" \
  "v_add_u32 %19, %24, %19\n\t" \
  "v_mfma_f32_16x16x32_bf16 %6, v[220:223], v[200:203], 0\n\t" \
  "v_add_u32 %20, %24, %20\n\t" \
  "v_mfma_f32_16x16x32_bf16 %7, v[220:223], v[204:207], 0\n\t" \
  "v_add_u32 %21, %24, %21\n\t" \
  "v_mfma_f32_16x16x32_bf16 %8, v[220:223], v[208:211], 0\n\t" \
  "v_add_u32 %22, %24, %22\n\t" \
  "v_mfma_f32_16x16x32_bf16 %9, v[224:227], v[200:203], 0\n\t" \
  "v_add_u32 %23, %24, %23\n\t" \
  "v_mfma_f32_16x16x32_bf16 %10, v[224:227], v[204:207], 0\n\t" \
  "v_mfma_f32_16x16x32_bf16 %11, v[224:227], v[208:211], 0\n\t" \
  "s_waitcnt lgkmcnt(0)\n\t" \
  "s_barrier\n\t" \
  "v_mfma_f32_16x16x32_bf16 %0, v[240:243], v[228:231], %0\n\t" \
  "ds_read_b128 v[212:215], %22 offset:0\n\t" \
  "ds_read_b64_tr_b16 v[200:201], %19 offset:0\n\t" \
  "v_mfma_f32_16x16x32_bf16 %1, v[240:243], v[232:235], %1\n\t" \
  "ds_read_b64_tr_b16 v[202:203], %19 offset:1280\n\t" \
  "ds_read_b64_tr_b16 v[204:205], %20 offset:0\n\t" \
  "v_mfma_f32_16x16x32_bf16 %2, v[240:243], v[236:239], %2\n\t" \
  "ds_read_b64_tr_b16 v[206:207], %20 offset:1280\n\t" \
  "ds_read_b64_tr_b16 v[208:209], %21 offset:0\n\t" \
  "v_mfma_f32_16x16x32_bf16 %3, v[244:247], v[228:231], %3\n\t" \
  "ds_read_b64_tr_b16 v[210:211], %21 offset:1280\n\t" \
  "ds_read_b128 v[216:219], %22 offset:2048\n\t" \
  "v_mfma_f32_16x16x32_bf16 %4, v[244:247], v[232:235], %4\n\t" \
  "ds_read_b128 v[220:223], %22 offset:4096\n\t" \
  "ds_read_b128 v[224:227], %22 offset:6144\n\t" \
  "v_mfma_f32_16x16x32_bf16 %5, v[244:247], v[236:239], %5\n\t" \
  "v_mfma_f32_16x16x32_bf16 %6, v[248:251], v[228:231], %6\n\t" \
  "v_mfma_f32_16x16x32_bf16 %7, v[248:251], v[232:235], %7\n\t" \
  "v_mfma_f32_16x16x32_bf16 %8, v[248:251], v[236:239], %8\n\t" \
  "v_mfma_f32_16x16x32_bf16 %9, v[252:255], v[228:231], %9\n\t" \
  "v_mfma_f32_16x16x32_bf16 %10, v[252:255], v[232:235], %10\n\t" \
  "v_mfma_f32_16x16x32_bf16 %11, v[252:255], v[236:239], %11\n\t" \
  "s_waitcnt lgkmcnt(0)\n\t" \
  "s_nop 7\n\t"

#define ASM_LOAD_A \
  "ds_read_b64_tr_b16 v[200:201], %7 offset:0\n\t" \
  "ds_read_b64_tr_b16 v[202:203], %7 offset:1536\n\t" \
  "ds_read_b128 v[212:215], %10 offset:0\n\t" \
  "ds_read_b128 v[216:219], %10 offset:2048\n\t" \
  "ds_read_b128 v[220:223], %10 offset:4096\n\t" \
  "ds_read_b128 v[224:227], %10 offset:6144\n\t" \
  "ds_read_b64_tr_b16 v[204:205], %8 offset:0\n\t" \
  "ds_read_b64_tr_b16 v[206:207], %8 offset:1536\n\t" \
  "ds_read_b64_tr_b16 v[208:209], %9 offset:0\n\t" \
  "ds_read_b64_tr_b16 v[210:211], %9 offset:1536\n\t"

#define ASM_LOAD_B \
  "ds_read_b128 v[212:215], %10 offset:0\n\t" \
  "ds_read_b64_tr_b16 v[200:201], %7 offset:0\n\t" \
  "ds_read_b64_tr_b16 v[202:203], %7 offset:1280\n\t" \
  "ds_read_b64_tr_b16 v[204:205], %8 offset:0\n\t" \
  "ds_read_b64_tr_b16 v[206:207], %8 offset:1280\n\t" \
  "ds_read_b64_tr_b16 v[208:209], %9 offset:0\n\t" \
  "ds_read_b64_tr_b16 v[210:211], %9 offset:1280\n\t" \
  "ds_read_b128 v[216:219], %10 offset:2048\n\t" \
  "ds_read_b128 v[220:223], %10 offset:4096\n\t" \
  "ds_read_b128 v[224:227], %10 offset:6144\n\t"

struct Geo { unsigned b, g, j, h, c0; };

DI void dma_wave_w(const Geo& G, unsigned dw, const void* kc, const void* wq, const void* wo, unsigned lane, unsigned wave) {
  const __amdgpu_buffer_rsrc_t rA = __builtin_amdgcn_make_buffer_rsrc((void*)wq, 0, DM * NH * DH * 2, 0x00020000);
  unsigned vA[6], rr[3], cc[3];
#pragma unroll
  for (int m = 0; m < 3; ++m) {
    const unsigned u = 64 * m + lane, r = (u * 2731) >> 16, c = u - 24 * r;
    rr[m] = r; cc[m] = c;
    vA[m] = r * 12288 + ((c ^ (r & 2)) << 4);
    vA[m + 3] = (vA[m] ^ 64) + 8 * 12288;
  }
  unsigned kcx = G.c0;
  auto issueA = [&](unsigned n) {
    const unsigned slot = (n & 3) * SLOT + dw * 12288, so = (kcx * 64 + dw * 32) * 12288 + G.h * 384;
#pragma unroll
    for (int p = 0; p < 12; ++p) dma16(rA, slot + p * 1024, vA[p % 6], so + (p / 6) * 16 * 12288);
    kcx = kcx == 9 ? 0u : kcx + 1;
  };
#pragma nounroll
  for (unsigned n = 0; n < 3; ++n) issueA(n);
  const __amdgpu_buffer_rsrc_t rK = __builtin_amdgcn_make_buffer_rsrc((void*)kc, 0, NB * NH * SK * DH * 2, 0x00020000);
  const __amdgpu_buffer_rsrc_t rB = __builtin_amdgcn_make_buffer_rsrc((void*)wo, 0, DM * NH * DH * 2, 0x00020000);
  unsigned vK[6], vB[5];
#pragma unroll
  for (int m = 0; m < 3; ++m) {
    vK[m] = rr[m] * 384 + ((cc[m] ^ (rr[m] >> 1)) << 4);
    vK[m + 3] = (vK[m] ^ 64) + 8 * 384;
  }
#pragma unroll
  for (int m = 0; m < 5; ++m) {
    const unsigned u = 64 * m + lane, r = (u * 3277) >> 16, c = u - 20 * r;
    vB[m] = r * 1280 + ((c ^ ((r >> 2) & 2)) << 4);
  }
  unsigned row = 192 * G.j;
  const unsigned sb = (768 * G.g + dw * 32) * 1280 + 320 * G.j;
  auto issueB = [&](unsigned n) {
    const unsigned slot = (n & 3) * SLOT + dw * 10240, so = sb + row * 1280;
#pragma unroll
    for (int p = 0; p < 10; ++p) dma16(rB, slot + p * 1024, vB[p % 5], so + (p / 5) * 16 * 1280);
    row = row == 704 ? 0u : row + 64;
  };
  WAITVM(24); BAR();
  STAMP(1);
#pragma nounroll
  for (unsigned t = 0; t < 7; ++t) { issueA(t + 3); STAMPD(16 + 4 * t); WAITVM(24); STAMPD(17 + 4 * t); BAR(); }
  { const unsigned slot = (10 & 3) * SLOT + dw * 12288, so = ((G.b * 32 + G.h) * 64 + dw * 32) * 384;
#pragma unroll
    for (int p = 0; p < 12; ++p) dma16(rK, slot + p * 1024, vK[p % 6], so + (p / 6) * 16 * 384); }
  WAITVM(24); BAR();
#pragma nounroll
  for (unsigned t = 8; t < 20; ++t) {
    if (t == 10) BAR();
    issueB(t + 3);
    STAMPD(16 + 4 * t);
    if (t == 8) WAITVM(22); else WAITVM(20);
    STAMPD(17 + 4 * t);
    BAR();
  }
  WAITVM(10); BAR();
  WAITVM(0); BAR();
  BAR();
  STAMP(3);
}
DI void dma_wave_a(const Geo& G, const void* x, const void* vc, unsigned lane, unsigned wave) {
  const __amdgpu_buffer_rsrc_t rA = __builtin_amdgcn_make_buffer_rsrc((void*)x, 0, NB * SQ * DM * 2, 0x00020000);
  const unsigned r = lane >> 3, c = lane & 7, cs = (c ^ (r >> 1)) << 4;
  const unsigned vA0 = r * 1280 + cs, vA1 = r * 1280 + (cs ^ 64);
  unsigned kcx = G.c0;
  auto issueA = [&](unsigned n) {
    const unsigned slot = (n & 3) * SLOT + AUX, so = G.b * 64 * 1280 + kcx * 128;
#pragma unroll
    for (int p = 0; p < 8; ++p) dma16(rA, slot + p * 1024, (p & 1) ? vA1 : vA0, so + p * 8 * 1280);
    kcx = kcx == 9 ? 0u : kcx + 1;
  };
#pragma nounroll
  for (unsigned n = 0; n < 3; ++n) issueA(n);
  const __amdgpu_buffer_rsrc_t rB = __builtin_amdgcn_make_buffer_rsrc((void*)vc, 0, NB * NH * SK * DH * 2, 0x00020000);
  const unsigned vB0 = r * 384 + cs, vB1 = r * 384 + (cs ^ 64);
  unsigned hh = G.j, dc = 0;
  auto issueB = [&](unsigned n) {
    const unsigned slot = (n & 3) * SLOT + AUX, so = (G.b * 32 + 4 * G.g + hh) * 24576 + dc * 128;
#pragma unroll
    for (int p = 0; p < 8; ++p) dma16(rB, slot + p * 1024, (p & 1) ? vB1 : vB0, so + p * 8 * 384);
    hh = dc == 2 ? (hh + 1) & 3 : hh;
    dc = dc == 2 ? 0u : dc + 1;
  };
  WAITVM(16); BAR();
#pragma nounroll
  for (unsigned t = 0; t < 7; ++t) { issueA(t + 3); WAITVM(16); BAR(); }
  WAITVM(8); BAR();
#pragma nounroll
  for (unsigned t = 8; t < 20; ++t) {
    if (t == 10) BAR();
    issueB(t + 3);
    if (t == 8) WAITVM(8); else WAITVM(16);
    BAR();
  }
  WAITVM(8); BAR();
  WAITVM(0); BAR();
  BAR();
  STAMP(3);
}

DI void reduce_tile(const Geo& G, unsigned dt, u64 tagR, char* ws, void* out, unsigned lane, bool upd) {
  const unsigned nt = dt >> 1, ip = dt & 1;
  const unsigned pw = nt < 3 ? 0u : nt < 6 ? 1u : nt < 8 ? 2u : 3u;
  const char* fl = ws + WS_FR + (size_t)((((G.b * 4 + G.j) * 8 + (lane & 7)) * 4 + pw) * 128);
#pragma nounroll
  for (unsigned sp = 0; sp < SPIN_CAP; ++sp) {
    u64 v = ld_sc1_64(fl);
    if (__all(v == tagR)) break;
  }
  CBAR();
  __amdgpu_buffer_rsrc_t rR = __builtin_amdgcn_make_buffer_rsrc((void*)(ws + WS_R), 0, 256 * 20480, 0x00020000);
  const unsigned base = ((G.b * 4 + G.j) * 8) * 20480 + dt * 1024;
  u32x4 v[8];
#pragma unroll
  for (int s = 0; s < 8; ++s) v[s] = __builtin_amdgcn_raw_buffer_load_b128(rR, lane * 16, base + s * 20480, LD_AUX);
  float a[8];
#pragma unroll
  for (int k = 0; k < 4; ++k) { a[2 * k] = __uint_as_float(v[0][k] << 16); a[2 * k + 1] = __uint_as_float(v[0][k] & 0xffff0000u); }
#pragma unroll
  for (int s = 1; s < 8; ++s)
#pragma unroll
    for (int k = 0; k < 4; ++k) { a[2 * k] += __uint_as_float(v[s][k] << 16); a[2 * k + 1] += __uint_as_float(v[s][k] & 0xffff0000u); }
  __amdgpu_buffer_rsrc_t rO = __builtin_amdgcn_make_buffer_rsrc(out, 0, NB * SQ * DM * 2, 0x00020000);
  const unsigned row = G.b * 64 + 32 * ip + (lane & 15), col = 160 * G.j + 16 * nt + 4 * (lane >> 4);
  u32x2 o0 = {cvtpk(a[0], a[1]), cvtpk(a[2], a[3])}, o1 = {cvtpk(a[4], a[5]), cvtpk(a[6], a[7])};
  __builtin_amdgcn_raw_buffer_store_b64(o0, rO, (row * DM + col) * 2, 0, 0);
  __builtin_amdgcn_raw_buffer_store_b64(o1, rO, ((row + 16) * DM + col) * 2, 0, 0);
  if (upd && lane == 0) st_sc1_64(ws + WS_ER + (G.b * 4 + G.j) * 128, tagR);
}

struct Frag { u32x4 f[7]; };
#define F0_OPS(F) "+{v[200:203]}"(F.f[0]), "+{v[204:207]}"(F.f[1]), "+{v[208:211]}"(F.f[2]), "+{v[212:215]}"(F.f[3]), "+{v[216:219]}"(F.f[4]), "+{v[220:223]}"(F.f[5]), "+{v[224:227]}"(F.f[6])
#define F1_CLOB "v228", "v229", "v230", "v231", "v232", "v233", "v234", "v235", "v236", "v237", "v238", "v239", "v240", "v241", "v242", "v243", "v244", "v245", "v246", "v247", "v248", "v249", "v250", "v251", "v252", "v253", "v254", "v255"
#define RUN_CHUNK(TXT, acc, F, ad, delta) asm volatile(TXT \
    : "+v"(acc[0]), "+v"(acc[1]), "+v"(acc[2]), "+v"(acc[3]), "+v"(acc[4]), "+v"(acc[5]), "+v"(acc[6]), "+v"(acc[7]), "+v"(acc[8]), "+v"(acc[9]), "+v"(acc[10]), "+v"(acc[11]), \
      F0_OPS(F), "+v"(ad[0]), "+v"(ad[1]), "+v"(ad[2]), "+v"(ad[3]), "+v"(ad[4]) : "s"(delta) : F1_CLOB, "memory")
#define RUN_CHUNK0(TXT, acc, F, ad, delta) asm volatile(TXT \
    : "=&v"(acc[0]), "=&v"(acc[1]), "=&v"(acc[2]), "=&v"(acc[3]), "=&v"(acc[4]), "=&v"(acc[5]), "=&v"(acc[6]), "=&v"(acc[7]), "=&v"(acc[8]), "=&v"(acc[9]), "=&v"(acc[10]), "=&v"(acc[11]), \
      F0_OPS(F), "+v"(ad[0]), "+v"(ad[1]), "+v"(ad[2]), "+v"(ad[3]), "+v"(ad[4]) : "s"(delta) : F1_CLOB, "memory")
#define RUN_LOAD(TXT, F, ad) asm volatile(TXT : F0_OPS(F) : "v"(ad[0]), "v"(ad[1]), "v"(ad[2]), "v"(ad[3]) : "memory")
DI unsigned slot_delta(unsigned c) { return (c & 3) == 3 ? (unsigned)(-3 * (int)SLOT) : SLOT; }

__global__ void __launch_bounds__(512, 2)
mha_fused(const void* __restrict__ x, const void* __restrict__ kc, const void* __restrict__ vc, const void* __restrict__ wq,
          const void* __restrict__ wo, void* __restrict__ out, char* __restrict__ ws) {
  const unsigned tid = threadIdx.x, lane = tid & 63, wave = rfl(tid >> 6);
  Geo G;
  { const unsigned bid = blockIdx.x; G.g = bid & 7; const unsigned idx = bid >> 3; G.j = idx & 3; G.b = idx >> 2; G.h = 4 * G.g + G.j;
    const unsigned fb = (G.b * 10) >> 3, oj = (G.j * 10 + 2) >> 2;
    unsigned c0 = fb + oj; if (c0 >= 10) c0 -= 10; G.c0 = ROTATE ? c0 : 0u; }
  STAMP_BEGIN();
  if (wave >= 4 && wave < 7) {
    if (wave < 6) dma_wave_w(G, wave - 4, kc, wq, wo, lane, wave);
    else dma_wave_a(G, x, vc, lane, wave);
    const u64 tagR = lds_u64(MISC + L_TAGR);
    const unsigned dt = G.g + 8 * (wave - 4);
    if (dt < 20) reduce_tile(G, dt, tagR, ws, out, lane, wave == 4 && G.g == 0);
    STAMP(5);
  } else if (wave == 7) {
    const u64 eP = ld_sc1_64(ws + WS_EP + (G.b * 8 + G.g) * 128);
    const u64 eR = ld_sc1_64(ws + WS_ER + (G.b * 4 + G.j) * 128);
    BAR(); BAR(); BAR(); BAR(); BAR();
    const u64 tagP = next_tag(eP), tagR = next_tag(eR);
    if (lane == 0) { lds_st32(MISC + L_TAGP, (unsigned)tagP); lds_st32(MISC + L_TAGP + 4, (unsigned)(tagP >> 32));
                     lds_st32(MISC + L_TAGR, (unsigned)tagR); lds_st32(MISC + L_TAGR + 4, (unsigned)(tagR >> 32)); }
    BAR_L(); BAR(); BAR(); BAR(); BAR(); BAR();
    BAR(); BAR(); BAR(); BAR();
    STAMP(1);
    const __amdgpu_buffer_rsrc_t rP = __builtin_amdgcn_make_buffer_rsrc((void*)(ws + WS_P), 0, 256 * 8192, 0x00020000);
    const unsigned pl = opaque(PBUF + lane * 16);
    const char* fl = ws + WS_FP + (size_t)((G.b * 32 + 4 * G.g) * 4 + (lane & 15)) * 128;
    unsigned s = 13;
    {
      u64 pv;
      asm volatile("global_load_dwordx2 %0, %1, off sc1" : "=v"(pv) : "v"(fl) : "memory");
#pragma nounroll
      for (unsigned sp = 0; sp < SPIN_CAP; ++sp) {
        if (s == 20 || vm_idle_soon()) {
          asm volatile("s_waitcnt vmcnt(0)" : "+v"(pv) :: "memory");
          if (__all(pv == tagP)) break;
          asm volatile("global_load_dwordx2 %0, %1, off sc1" : "=v"(pv) : "v"(fl) : "memory");
        }
        if (s < 20) { BAR(); ++s; }
      }
      asm volatile("s_waitcnt vmcnt(0)" : "+v"(pv) :: "memory");
    }
    STAMP(2);
    u32x4 f[3][8];
#pragma unroll
    for (int u = 0; u < 3; ++u) {
      const unsigned hh = (G.j + u + 1) & 3;
#pragma unroll
      for (int i = 0; i < 8; ++i) f[u][i] = __builtin_amdgcn_raw_buffer_load_b128(rP, lane * 16, (G.b * 32 + 4 * G.g + hh) * 8192 + i * 1024, LD_AUX);
    }
    if (G.j == 0 && lane == 0) st_sc1_64(ws + WS_EP + (G.b * 8 + G.g) * 128, tagP);
#pragma nounroll
    while (s < 20 && !vm_idle_soon()) { BAR(); ++s; }
    STAMP(3);
#pragma unroll
    for (int i = 0; i < 8; ++i) *(LAS u32x4*)(uintptr_t)(pl + (i >> 1) * 6144 + 2048 + (i & 1) * 1024) = f[0][i];
    if (s < 20) { BAR_L(); ++s; }
#pragma unroll
    for (int i = 0; i < 8; ++i) *(LAS u32x4*)(uintptr_t)(pl + (i >> 1) * 6144 + 4096 + (i & 1) * 1024) = f[1][i];
    if (s < 20) { BAR_L(); ++s; }
#pragma nounroll
    while (s < 15) { BAR(); ++s; }
#pragma unroll
    for (int i = 0; i < 8; ++i) *(LAS u32x4*)(uintptr_t)(pl + (i >> 1) * 6144 + (i & 1) * 1024) = f[2][i];
    STAMP(4);
#pragma nounroll
    while (s <= 20) { BAR_L(); ++s; }
    BAR(); BAR();
    STAMP(6);
  } else {
    const unsigned i16 = lane & 15, g4 = lane >> 4, q = i16 >> 2, p = lane & 3;
    const unsigned a0 = AUX + i16 * 128 + ((g4 ^ (i16 >> 1)) << 4);
    Frag F0;
#pragma unroll
    for (int i = 0; i < 7; ++i) F0.f[i] = (u32x4){0, 0, 0, 0};
    unsigned ad[5];
    f32x4 acc[12];
#pragma unroll
    for (int t = 0; t < 12; ++t) acc[t] = (f32x4){0.f, 0.f, 0.f, 0.f};
    { const unsigned phi = ((q >> 1) & 1) | ((g4 & 1) << 1);
#pragma unroll
      for (int e = 0; e < 3; ++e) ad[e] = (8 * g4 + q) * 384 + 32 * ((3 * wave + e) ^ phi) + 16 * (p >> 1) + 8 * (p & 1);
      ad[3] = a0; ad[4] = a0 ^ 64; }
    asm volatile("" : "+v"(acc[0]), "+v"(acc[1]), "+v"(acc[2]), "+v"(acc[3]), "+v"(acc[4]), "+v"(acc[5]), "+v"(acc[6]), "+v"(acc[7]), "+v"(acc[8]), "+v"(acc[9]), "+v"(acc[10]), "+v"(acc[11]),
                      "+v"(ad[0]), "+v"(ad[1]), "+v"(ad[2]), "+v"(ad[3]), "+v"(ad[4]));
    BAR();
    STAMP(1);
    RUN_LOAD(ASM_LOAD_A, F0, ad);
#pragma nounroll
    for (unsigned c = 0; c < 10; ++c) { const unsigned dl = slot_delta(c); RUN_CHUNK(ASM_CHUNK_A, acc, F0, ad, dl); }
    STAMP(2);
    {
      const unsigned qw = opaque(PBUF + ((g4 >> 1) * 16 + i16) * 16 + (g4 & 1) * 8);
#pragma unroll
      for (int e = 0; e < 3; ++e) {
        const unsigned tau = 3 * wave + e;
        const unsigned qe = qw + (tau >> 1) * 1024 + (tau & 1) * 512;
#pragma unroll
        for (int t = 0; t < 4; ++t) { u32x2 v = {cvtpk(acc[4 * e + t][0], acc[4 * e + t][1]), cvtpk(acc[4 * e + t][2], acc[4 * e + t][3])};
          *(LAS u32x2*)(uintptr_t)(qe + t * 6144) = v; }
      }
    }
    BAR_L();
    STAMP(3);
    f32x4 st[4];
    {
      const unsigned qr = opaque(PBUF + wave * 6144 + lane * 16);
      bf16x8 qf[6];
#pragma unroll
      for (int s = 0; s < 6; ++s) qf[s] = lds_b128(qr + s * 1024);
      const unsigned slot = (10 & 3) * SLOT, fk = i16 >> 1;
      unsigned ka[2];
#pragma unroll
      for (int so = 0; so < 2; ++so) ka[so] = opaque(slot + i16 * 384 + (((4 * so + g4) ^ fk) << 4));
      bf16x8 kf[6][4];
#pragma unroll
      for (int s = 0; s < 6; ++s)
#pragma unroll
        for (int kt = 0; kt < 4; ++kt) kf[s][kt] = lds_b128(ka[s & 1] + kt * 16 * 384 + 128 * (s >> 1));
      BAR_L();
      SB0();
#pragma unroll
      for (int kt = 0; kt < 4; ++kt) st[kt] = (f32x4){0.f, 0.f, 0.f, 0.f};
#pragma unroll
      for (int s = 0; s < 6; ++s)
#pragma unroll
        for (int kt = 0; kt < 4; ++kt) st[kt] = MFMA(kf[s][kt], qf[s], st[kt]);
    }
    SB0();
    const unsigned ntb = wave == 3 ? 7u : 3u * wave;
    { const unsigned slot = (11 & 3) * SLOT;
#pragma unroll
      for (int e = 0; e < 3; ++e) ad[e] = slot + (8 * g4 + q) * 320 + 32 * ((ntb + e) ^ (g4 & 1)) + 16 * (p >> 1) + 8 * (p & 1);
      ad[3] = slot + a0; ad[4] = slot + (a0 ^ 64); }
    RUN_LOAD(ASM_LOAD_B, F0, ad);
    asm volatile("s_nop 7" : "+v"(st[0]), "+v"(st[1]), "+v"(st[2]), "+v"(st[3]));
    const __amdgpu_buffer_rsrc_t rP = __builtin_amdgcn_make_buffer_rsrc((void*)(ws + WS_P), 0, 256 * 8192, 0x00020000);
    {
      float m = __builtin_amdgcn_fmed3f(st[0][0], st[0][1], __builtin_inff());
      m = max3f(m, st[0][2], st[0][3]);
#pragma unroll
      for (int kt = 1; kt < 4; ++kt) { m = max3f(m, st[kt][0], st[kt][1]); m = max3f(m, st[kt][2], st[kt][3]); }
      { auto r1 = __builtin_amdgcn_permlane16_swap(__float_as_uint(m), __float_as_uint(m), false, false);
        m = max3f(__uint_as_float(r1[0]), __uint_as_float(r1[1]), __uint_as_float(r1[1]));
        auto r2 = __builtin_amdgcn_permlane32_swap(__float_as_uint(m), __float_as_uint(m), false, false);
        m = max3f(__uint_as_float(r2[0]), __uint_as_float(r2[1]), __uint_as_float(r2[1])); }
      constexpr float SC = 0.07216878364870322f * 1.4426950408889634f;
      const float ml = m * SC;
      float sum = 0.f;
#pragma unroll
      for (int kt = 0; kt < 4; ++kt)
#pragma unroll
        for (int r = 0; r < 4; ++r) { const float e = __builtin_amdgcn_exp2f(__builtin_fmaf(st[kt][r], SC, -ml)); st[kt][r] = e; sum += e; }
      { auto r1 = __builtin_amdgcn_permlane16_swap(__float_as_uint(sum), __float_as_uint(sum), false, false);
        sum = __uint_as_float(r1[0]) + __uint_as_float(r1[1]);
        auto r2 = __builtin_amdgcn_permlane32_swap(__float_as_uint(sum), __float_as_uint(sum), false, false);
        sum = __uint_as_float(r2[0]) + __uint_as_float(r2[1]); }
      const float inv = __builtin_amdgcn_rcpf(sum);
#pragma unroll
      for (int kt = 0; kt < 4; ++kt) st[kt] *= inv;
      const unsigned pw = opaque(PBUF + wave * 6144 + lane * 16);
#pragma unroll
      for (int s2 = 0; s2 < 2; ++s2) {
        const u32x4 pf = __builtin_bit_cast(u32x4, pack8(st[2 * s2], st[2 * s2 + 1]));
        __builtin_amdgcn_raw_buffer_store_b128(pf, rP, lane * 16, (G.b * 32 + G.h) * 8192 + (2 * wave + s2) * 1024, ST_AUX);
        *(LAS u32x4*)(uintptr_t)(pw + s2 * 1024) = pf;
      }
    }
    STAMP(4);
    f32x4 oa[4][3];
#pragma unroll
    for (int it = 0; it < 4; ++it)
#pragma unroll
      for (int e = 0; e < 3; ++e) oa[it][e] = (f32x4){0.f, 0.f, 0.f, 0.f};
    auto pvw = [&](unsigned buf, const bf16x8 (&pk)[2][3]) {
      const unsigned pb = opaque(PBUF + buf * 2048 + lane * 16);
      bf16x8 pf[4][2];
#pragma unroll
      for (int s2 = 0; s2 < 2; ++s2)
#pragma unroll
        for (int it = 0; it < 4; ++it) pf[it][s2] = lds_b128(pb + it * 6144 + s2 * 1024);
      SB0();
#pragma unroll
      for (int s2 = 0; s2 < 2; ++s2)
#pragma unroll
        for (int it = 0; it < 4; ++it)
#pragma unroll
          for (int e = 0; e < 3; ++e) oa[it][e] = MFMA(pk[s2][e], pf[it][s2], oa[it][e]);
      SB0();
    };
    f32x4 vw[12];
    auto pack_vw = [&](bf16x8 (&pk)[2][3]) {
#pragma unroll
      for (int s2 = 0; s2 < 2; ++s2)
#pragma unroll
        for (int e = 0; e < 3; ++e) pk[s2][e] = pack8(vw[3 * (2 * s2) + e], vw[3 * (2 * s2 + 1) + e]);
    };
    auto head_first = [&](unsigned c) { const unsigned dl = slot_delta(c); RUN_CHUNK0(ASM_CHUNK_B0, vw, F0, ad, dl); };
    auto head_next = [&](unsigned c) { const unsigned dl = slot_delta(c); RUN_CHUNK(ASM_CHUNK_B, vw, F0, ad, dl); };
    bf16x8 pk1[2][3], pk2[2][3];
    head_first(11);
    head_next(12);
    {
      const u64 tagP = lds_u64(MISC + L_TAGP);
      WAITVM(0);
      if (lane == 0) st_sc1_64(ws + WS_FP + (size_t)((G.b * 32 + G.h) * 4 + wave) * 128, tagP); }
    head_next(13);
    STAMP(5);
    pack_vw(pk1); pvw(0, pk1);
    head_first(14); head_next(15); head_next(16);
    STAMP(6);
    pack_vw(pk1);
    head_first(17); head_next(18); head_next(19);
    STAMP(7);
    pack_vw(pk2);
    head_first(20);
    pvw(1, pk1);
    head_next(21);
    pvw(2, pk2);
    head_next(22);
    STAMP(8);
    pack_vw(pk1); pvw(0, pk1);
    STAMP(9);
    const u64 tagR = lds_u64(MISC + L_TAGR);
    __amdgpu_buffer_rsrc_t rR = __builtin_amdgcn_make_buffer_rsrc((void*)(ws + WS_R), 0, 256 * 20480, 0x00020000);
    const unsigned base = ((G.b * 4 + G.j) * 8 + G.g) * 20480;
#pragma unroll
    for (int e = 0; e < 3; ++e) {
      const bool keep = wave < 2 || (wave == 2 ? e < 2 : e > 0);
      if (keep) {
#pragma unroll
        for (int ip = 0; ip < 2; ++ip)
          __builtin_amdgcn_raw_buffer_store_b128(__builtin_bit_cast(u32x4, pack8(oa[2 * ip][e], oa[2 * ip + 1][e])), rR, lane * 16, base + (2 * (ntb + e) + ip) * 1024, ST_AUX);
      }
    }
    WAITVM(0);
    if (lane == 0) st_sc1_64(ws + WS_FR + (size_t)((((G.b * 4 + G.j) * 8 + G.g) * 4 + wave) * 128), tagR);
    STAMP(10);
  }
  STAMP_END();
}

extern "C" void kernel_launch(void* const* d_in, const int* in_sizes, int n_in, void* d_out, int out_size, void* d_ws, size_t ws_size,
                              hipStream_t stream) {
  (void)in_sizes; (void)n_in; (void)out_size; (void)ws_size;
  (void)hipFuncSetAttribute((const void*)mha_fused, hipFuncAttributeMaxDynamicSharedMemorySize, LDS_BYTES);
  mha_fused<<<256, 512, LDS_BYTES, stream>>>(d_in[0], d_in[1], d_in[2], d_in[3], d_in[4], d_out, (char*)d_ws);
}
```

```cpp
#include <hip/hip_runtime.h>
#include <hip/hip_bf16.h>
#include <stdint.h>

#define LAS __attribute__((address_space(3)))
#define GAS __attribute__((address_space(1)))
#define DI __device__ __forceinline__
typedef short s16x4 __attribute__((ext_vector_type(4)));
typedef __bf16 bf16x2 __attribute__((ext_vector_type(2)));
typedef __bf16 bf16x8 __attribute__((ext_vector_type(8)));
typedef float f32x2 __attribute__((ext_vector_type(2)));
typedef float f32x4 __attribute__((ext_vector_type(4)));
typedef unsigned u32x2 __attribute__((ext_vector_type(2)));
typedef unsigned u32x4 __attribute__((ext_vector_type(4)));
typedef unsigned long long u64;

constexpr int NB = 8, SQ = 64, DM = 640, NH = 32, SK = 64, DH = 192;
constexpr int NCH = 23;
constexpr unsigned SLOT = 32768, AUX = 24576;
constexpr unsigned PBUF = 131072, MISC = PBUF + 3 * 8192, LDS_USED = MISC + 256;
constexpr unsigned L_TAGP = 0, L_TAGR = 8;
constexpr unsigned WS_EP = 0;
constexpr unsigned WS_ER = 8192;
constexpr unsigned WS_FP = 16384;
constexpr unsigned WS_FR = WS_FP + 131072;
constexpr unsigned WS_P = 524288;
constexpr unsigned WS_R = WS_P + 256 * 8192;
constexpr unsigned WS_DBG = 16u << 20;
constexpr unsigned SPIN_CAP = 1u << 16;

#define STAMPS 0
#define ROTATE 1
#define PRETOUCH 1
#define CODEPF 23000
#define IN_AUX 0
#define ST_AUX 16
#define LD_AUX 16
constexpr unsigned LDS_BYTES = LDS_USED + (STAMPS ? 4096 : 0);

DI unsigned rfl(unsigned x) { return __builtin_amdgcn_readfirstlane(x); }
DI unsigned cvtpk(float lo, float hi) { f32x2 v = {lo, hi}; bf16x2 b = __builtin_convertvector(v, bf16x2); return __builtin_bit_cast(unsigned, b); }
DI bf16x8 pack8(const f32x4& a, const f32x4& b) {
  u32x4 p = {cvtpk(a[0], a[1]), cvtpk(a[2], a[3]), cvtpk(b[0], b[1]), cvtpk(b[2], b[3])};
  return __builtin_bit_cast(bf16x8, p);
}
#define MFMA(a, b, c) __builtin_amdgcn_mfma_f32_16x16x32_bf16((a), (b), (c), 0, 0, 0)
DI bf16x8 lds_b128(unsigned addr) { return *(const LAS bf16x8*)(uintptr_t)addr; }
DI unsigned lds_u32(unsigned addr) { return *(const volatile LAS unsigned*)(uintptr_t)addr; }
DI void lds_st32(unsigned addr, unsigned v) { *(volatile LAS unsigned*)(uintptr_t)addr = v; }
DI u64 lds_u64(unsigned addr) { return (u64)lds_u32(addr) | ((u64)lds_u32(addr + 4) << 32); }
#define CBAR() asm volatile("" ::: "memory")
#define SB0() __builtin_amdgcn_sched_barrier(0)
#define BAR() asm volatile("s_barrier" ::: "memory")
#define BAR_L() asm volatile("s_waitcnt lgkmcnt(0)\n\ts_barrier" ::: "memory")
#define WAITVM(n) asm volatile("s_waitcnt vmcnt(" #n ")" ::: "memory")
DI void dma16(__amdgpu_buffer_rsrc_t r, unsigned lds, unsigned voff, unsigned soff) {
  __builtin_amdgcn_raw_ptr_buffer_load_lds(r, (LAS void*)(uintptr_t)lds, 16, voff, soff, 0, IN_AUX);
}
DI unsigned opaque(unsigned x) { asm volatile("" : "+v"(x)); return x; }
DI float max3f(float a, float b, float c) { float r; asm("v_max3_f32 %0, %1, %2, %3" : "=v"(r) : "v"(a), "v"(b), "v"(c)); return r; }
DI u64 next_tag(u64 e) { return e * 6364136223846793005ull + 1442695040888963407ull; }
DI u64 ld_sc1_64(const void* p) { return __hip_atomic_load((const GAS u64*)p, __ATOMIC_RELAXED, __HIP_MEMORY_SCOPE_AGENT); }
DI void st_sc1_64(void* p, u64 v) { __hip_atomic_store((GAS u64*)p, v, __ATOMIC_RELAXED, __HIP_MEMORY_SCOPE_AGENT); }
DI bool vm_idle() { unsigned r; asm volatile("s_getreg_b32 %0, hwreg(HW_REG_IB_STS)" : "=s"(r) :: "memory"); return (r & 0x00C0000Fu) == 0; }
DI bool vm_idle_soon() {
#pragma nounroll
  for (int k = 0; k < 4; ++k) { if (vm_idle()) return true; __builtin_amdgcn_s_sleep(1); }
  return false;
}
#define STAMP(i) do {} while (0)
#define STAMP_BEGIN() do {} while (0)
#define STAMP_END() do {} while (0)
#define STAMPD(i) do {} while (0)

#define ASM_CHUNK_A \
  "s_nop 1\n\t" \
  "s_waitcnt lgkmcnt(0)\n\t" \
  "v_mfma_f32_16x16x32_bf16 %0, v[200:203], v[212:215], %0\n\t" \
  "ds_read_b64_tr_b16 v[228:229], %19 offset:12288\n\t" \
  "ds_read_b64_tr_b16 v[230:231], %19 offset:13824\n\t" \
  "v_mfma_f32_16x16x32_bf16 %1, v[200:203], v[216:219], %1\n\t" \
  "ds_read_b128 v[240:243], %23 offset:0\n\t" \
  "ds_read_b128 v[244:247], %23 offset:2048\n\t" \
  "v_mfma_f32_16x16x32_bf16 %2, v[200:203], v[220:223], %2\n\t" \
  "ds_read_b128 v[248:251], %23 offset:4096\n\t" \
  "ds_read_b128 v[252:255], %23 offset:6144\n\t" \
  "v_mfma_f32_16x16x32_bf16 %3, v[200:203], v[224:227], %3\n\t" \
  "ds_read_b64_tr_b16 v[232:233], %20 offset:12288\n\t" \
  "ds_read_b64_tr_b16 v[234:235], %20 offset:13824\n\t" \
  "v_mfma_f32_16x16x32_bf16 %4, v[204:207], v[212:215], %4\n\t" \
  "ds_read_b64_tr_b16 v[236:237], %21 offset:12288\n\t" \
  "ds_read_b64_tr_b16 v[238:239], %21 offset:13824\n\t" \
  "v_mfma_f32_16x16x32_bf16 %5, v[204:207], v[216:219], %5\n\t" \
  "v_add_u32 %19, %24, %19\n\t" \
  "v_mfma_f32_16x16x32_bf16 %6, v[204:207], v[220:223], %6\n\t" \
  "v_add_u32 %20, %24, %20\n\t" \
  "v_mfma_f32_16x16x32_bf16 %7, v[204:207], v[224:227], %7\n\t" \
  "v_add_u32 %21, %24, %21\n\t" \
  "v_mfma_f32_16x16x32_bf16 %8, v[208:211], v[212:215], %8\n\t" \
  "v_add_u32 %22, %24, %22\n\t" \
  "v_mfma_f32_16x16x32_bf16 %9, v[208:211], v[216:219], %9\n\t" \
  "v_add_u32 %23, %24, %23\n\t" \
  "v_mfma_f32_16x16x32_bf16 %10, v[208:211], v[220:223], %10\n\t" \
  "v_mfma_f32_16x16x32_bf16 %11, v[208:211], v[224:227], %11\n\t" \
  "s_waitcnt lgkmcnt(0)\n\t" \
  "s_barrier\n\t" \
  "v_mfma_f32_16x16x32_bf16 %0, v[228:231], v[240:243], %0\n\t" \
  "ds_read_b64_tr_b16 v[200:201], %19 offset:0\n\t" \
  "ds_read_b64_tr_b16 v[202:203], %19 offset:1536\n\t" \
  "v_mfma_f32_16x16x32_bf16 %1, v[228:231], v[244:247], %1\n\t" \
  "ds_read_b128 v[212:215], %22 offset:0\n\t" \
  "ds_read_b128 v[216:219], %22 offset:2048\n\t" \
  "v_mfma_f32_16x16x32_bf16 %2, v[228:231], v[248:251], %2\n\t" \
  "ds_read_b128 v[220:223], %22 offset:4096\n\t" \
  "ds_read_b128 v[224:227], %22 offset:6144\n\t" \
  "v_mfma_f32_16x16x32_bf16 %3, v[228:231], v[252:255], %3\n\t" \
  "ds_read_b64_tr_b16 v[204:205], %20 offset:0\n\t" \
  "ds_read_b64_tr_b16 v[206:207], %20 offset:1536\n\t" \
  "v_mfma_f32_16x16x32_bf16 %4, v[232:235], v[240:243], %4\n\t" \
  "ds_read_b64_tr_b16 v[208:209], %21 offset:0\n\t" \
  "ds_read_b64_tr_b16 v[210:211], %21 offset:1536\n\t" \
  "v_mfma_f32_16x16x32_bf16 %5, v[232:235], v[244:247], %5\n\t" \
  "v_mfma_f32_16x16x32_bf16 %6, v[232:235], v[248:251], %6\n\t" \
  "v_mfma_f32_16x16x32_bf16 %7, v[232:235], v[252:255], %7\n\t" \
  "v_mfma_f32_16x16x32_bf16 %8, v[236:239], v[240:243], %8\n\t" \
  "v_mfma_f32_16x16x32_bf16 %9, v[236:239], v[244:247], %9\n\t" \
  "v_mfma_f32_16x16x32_bf16 %10, v[236:239], v[248:251], %10\n\t" \
  "v_mfma_f32_16x16x32_bf16 %11, v[236:239], v[252:255], %11\n\t" \
  "s_waitcnt lgkmcnt(0)\n\t" \
  "s_nop 7\n\t"

#define ASM_CHUNK_B \
  "s_nop 1\n\t" \
  "s_waitcnt lgkmcnt(0)\n\t" \
  "v_mfma_f32_16x16x32_bf16 %0, v[212:215], v[200:203], %0\n\t" \
  "ds_read_b128 v[240:243], %23 offset:0\n\t" \
  "ds_read_b64_tr_b16 v[228:229], %19 offset:10240\n\t" \
  "v_mfma_f32_16x16x32_bf16 %1, v[212:215], v[204:207], %1\n\t" \
  "ds_read_b64_tr_b16 v[230:231], %19 offset:11520\n\t" \
  "ds_read_b64_tr_b16 v[232:233], %20 offset:10240\n\t" \
  "v_mfma_f32_16x16x32_bf16 %2, v[212:215], v[208:211], %2\n\t" \
  "ds_read_b64_tr_b16 v[234:235], %20 offset:11520\n\t" \
  "ds_read_b64_tr_b16 v[236:237], %21 offset:10240\n\t" \
  "v_mfma_f32_16x16x32_bf16 %3, v[216:219], v[200:203], %3\n\t" \
  "ds_read_b64_tr_b16 v[238:239], %21 offset:11520\n\t" \
  "ds_read_b128 v[244:247], %23 offset:2048\n\t" \
  "v_mfma_f32_16x16x32_bf16 %4, v[216:219], v[204:207], %4\n\t" \
  "ds_read_b128 v[248:251], %23 offset:4096\n\t" \
  "ds_read_b128 v[252:255], %23 offset:6144\n\t" \
  "v_mfma_f32_16x16x32_bf16 %5, v[216:219], v[208:211], %5\n\t" \
  "v_add_u32 %19, %24, %19\n\t" \
  "v_mfma_f32_16x16x32_bf16 %6, v[220:223], v[200:203], %6\n\t" \
  "v_add_u32 %20, %24, %20\n\t" \
  "v_mfma_f32_16x16x32_bf16 %7, v[220:223], v[204:207], %7\n\t" \
  "v_add_u32 %21, %24, %21\n\t" \
  "v_mfma_f32_16x16x32_bf16 %8, v[220:223], v[208:211], %8\n\t" \
  "v_add_u32 %22, %24, %22\n\t" \
  "v_mfma_f32_16x16x32_bf16 %9, v[224:227], v[200:203], %9\n\t" \
  "v_add_u32 %23, %24, %23\n\t" \
  "v_mfma_f32_16x16x32_bf16 %10, v[224:227], v[204:207], %10\n\t" \
  "v_mfma_f32_16x16x32_bf16 %11, v[224:227], v[208:211], %11\n\t" \
  "s_waitcnt lgkmcnt(0)\n\t" \
  "s_barrier\n\t" \
  "v_mfma_f32_16x16x32_bf16 %0, v[240:243], v[228:231], %0\n\t" \
  "ds_read_b128 v[212:215], %22 offset:0\n\t" \
  "ds_read_b64_tr_b16 v[200:201], %19 offset:0\n\t" \
  "v_mfma_f32_16x16x32_bf16 %1, v[240:243], v[232:235], %1\n\t" \
  "ds_read_b64_tr_b16 v[202:203], %19 offset:1280\n\t" \
  "ds_read_b64_tr_b16 v[204:205], %20 offset:0\n\t" \
  "v_mfma_f32_16x16x32_bf16 %2, v[240:243], v[236:239], %2\n\t" \
  "ds_read_b64_tr_b16 v[206:207], %20 offset:1280\n\t" \
  "ds_read_b64_tr_b16 v[208:209], %21 offset:0\n\t" \
  "v_mfma_f32_16x16x32_bf16 %3, v[244:247], v[228:231], %3\n\t" \
  "ds_read_b64_tr_b16 v[210:211], %21 offset:1280\n\t" \
  "ds_read_b128 v[216:219], %22 offset:2048\n\t" \
  "v_mfma_f32_16x16x32_bf16 %4, v[244:247], v[232:235], %4\n\t" \
  "ds_read_b128 v[220:223], %22 offset:4096\n\t" \
  "ds_read_b128 v[224:227], %22 offset:6144\n\t" \
  "v_mfma_f32_16x16x32_bf16 %5, v[244:247], v[236:239], %5\n\t" \
  "v_mfma_f32_16x16x32_bf16 %6, v[248:251], v[228:231], %6\n\t" \
  "v_mfma_f32_16x16x32_bf16 %7, v[248:251], v[232:235], %7\n\t" \
  "v_mfma_f32_16x16x32_bf16 %8, v[248:251], v[236:239], %8\n\t" \
  "v_mfma_f32_16x16x32_bf16 %9, v[252:255], v[228:231], %9\n\t" \
  "v_mfma_f32_16x16x32_bf16 %10, v[252:255], v[232:235], %10\n\t" \
  "v_mfma_f32_16x16x32_bf16 %11, v[252:255], v[236:239], %11\n\t" \
  "s_waitcnt lgkmcnt(0)\n\t" \
  "s_nop 7\n\t"

#define ASM_CHUNK_B0 \
  "s_nop 1\n\t" \
  "s_waitcnt lgkmcnt(0)\n\t" \
  "v_mfma_f32_16x16x32_bf16 %0, v[212:215], v[200:203], 0\n\t" \
  "ds_read_b128 v[240:243], %23 offset:0\n\t" \
  "ds_read_b64_tr_b16 v[228:229], %19 offset:10240\n\t" \
  "v_mfma_f32_16x16x32_bf16 %1, v[212:215], v[204:207], 0\n\t" \
  "ds_read_b64_tr_b16 v[230:231], %19 offset:11520\n\t" \
  "ds_read_b64_tr_b16 v[232:233], %20 offset:10240\n\t" \
  "v_mfma_f32_16x16x32_bf16 %2, v[212:215], v[208:211], 0\n\t" \
  "ds_read_b64_tr_b16 v[234:235], %20 offset:11520\n\t" \
  "ds_read_b64_tr_b16 v[236:237], %21 offset:10240\n\t" \
  "v_mfma_f32_16x16x32_bf16 %3, v[216:219], v[200:203], 0\n\t" \
  "ds_read_b64_tr_b16 v[238:239], %21 offset:11520\n\t" \
  "ds_read_b128 v[244:247], %23 offset:2048\n\t" \
  "v_mfma_f32_16x16x32_bf16 %4, v[216:219], v[204:207], 0\n\t" \
  "ds_read_b128 v[248:251], %23 offset:4096\n\t" \
  "ds_read_b128 v[252:255], %23 offset:6144\n\t" \
  "v_mfma_f32_16x16x32_bf16 %5, v[216:219], v[208:211], 0\n\t" \
  "v_add_u32 %19, %24, %19\n\t" \
  "v_mfma_f32_16x16x32_bf16 %6, v[220:223], v[200:203], 0\n\t" \
  "v_add_u32 %20, %24, %20\n\t" \
  "v_mfma_f32_16x16x32_bf16 %7, v[220:223], v[204:207], 0\n\t" \
  "v_add_u32 %21, %24, %21\n\t" \
  "v_mfma_f32_16x16x32_bf16 %8, v[220:223], v[208:211], 0\n\t" \
  "v_add_u32 %22, %24, %22\n\t" \
  "v_mfma_f32_16x16x32_bf16 %9, v[224:227], v[200:203], 0\n\t" \
  "v_add_u32 %23, %24, %23\n\t" \
  "v_mfma_f32_16x16x32_bf16 %10, v[224:227], v[204:207], 0\n\t" \
  "v_mfma_f32_16x16x32_bf16 %11, v[224:227], v[208:211], 0\n\t" \
  "s_waitcnt lgkmcnt(0)\n\t" \
  "s_barrier\n\t" \
  "v_mfma_f32_16x16x32_bf16 %0, v[240:243], v[228:231], %0\n\t" \
  "ds_read_b128 v[212:215], %22 offset:0\n\t" \
  "ds_read_b64_tr_b16 v[200:201], %19 offset:0\n\t" \
  "v_mfma_f32_16x16x32_bf16 %1, v[240:243], v[232:235], %1\n\t" \
  "ds_read_b64_tr_b16 v[202:203], %19 offset:1280\n\t" \
  "ds_read_b64_tr_b16 v[204:205], %20 offset:0\n\t" \
  "v_mfma_f32_16x16x32_bf16 %2, v[240:243], v[236:239], %2\n\t" \
  "ds_read_b64_tr_b16 v[206:207], %20 offset:1280\n\t" \
  "ds_read_b64_tr_b16 v[208:209], %21 offset:0\n\t" \
  "v_mfma_f32_16x16x32_bf16 %3, v[244:247], v[228:231], %3\n\t" \
  "ds_read_b64_tr_b16 v[210:211], %21 offset:1280\n\t" \
  "ds_read_b128 v[216:219], %22 offset:2048\n\t" \
  "v_mfma_f32_16x16x32_bf16 %4, v[244:247], v[232:235], %4\n\t" \
  "ds_read_b128 v[220:223], %22 offset:4096\n\t" \
  "ds_read_b128 v[224:227], %22 offset:6144\n\t" \
  "v_mfma_f32_16x16x32_bf16 %5, v[244:247], v[236:239], %5\n\t" \
  "v_mfma_f32_16x16x32_bf16 %6, v[248:251], v[228:231], %6\n\t" \
  "v_mfma_f32_16x16x32_bf16 %7, v[248:251], v[232:235], %7\n\t" \
  "v_mfma_f32_16x16x32_bf16 %8, v[248:251], v[236:239], %8\n\t" \
  "v_mfma_f32_16x16x32_bf16 %9, v[252:255], v[228:231], %9\n\t" \
  "v_mfma_f32_16x16x32_bf16 %10, v[252:255], v[232:235], %10\n\t" \
  "v_mfma_f32_16x16x32_bf16 %11, v[252:255], v[236:239], %11\n\t" \
  "s_waitcnt lgkmcnt(0)\n\t" \
  "s_nop 7\n\t"

#define ASM_LOAD_A \
  "ds_read_b64_tr_b16 v[200:201], %7 offset:0\n\t" \
  "ds_read_b64_tr_b16 v[202:203], %7 offset:1536\n\t" \
  "ds_read_b128 v[212:215], %10 offset:0\n\t" \
  "ds_read_b128 v[216:219], %10 offset:2048\n\t" \
  "ds_read_b128 v[220:223], %10 offset:4096\n\t" \
  "ds_read_b128 v[224:227], %10 offset:6144\n\t" \
  "ds_read_b64_tr_b16 v[204:205], %8 offset:0\n\t" \
  "ds_read_b64_tr_b16 v[206:207], %8 offset:1536\n\t" \
  "ds_read_b64_tr_b16 v[208:209], %9 offset:0\n\t" \
  "ds_read_b64_tr_b16 v[210:211], %9 offset:1536\n\t"

#define ASM_LOAD_B \
  "ds_read_b128 v[212:215], %10 offset:0\n\t" \
  "ds_read_b64_tr_b16 v[200:201], %7 offset:0\n\t" \
  "ds_read_b64_tr_b16 v[202:203], %7 offset:1280\n\t" \
  "ds_read_b64_tr_b16 v[204:205], %8 offset:0\n\t" \
  "ds_read_b64_tr_b16 v[206:207], %8 offset:1280\n\t" \
  "ds_read_b64_tr_b16 v[208:209], %9 offset:0\n\t" \
  "ds_read_b64_tr_b16 v[210:211], %9 offset:1280\n\t" \
  "ds_read_b128 v[216:219], %10 offset:2048\n\t" \
  "ds_read_b128 v[220:223], %10 offset:4096\n\t" \
  "ds_read_b128 v[224:227], %10 offset:6144\n\t"

struct Geo { unsigned b, g, j, h, c0; };

DI void dma_wave_w(const Geo& G, unsigned dw, const void* kc, const void* wq, const void* wo, unsigned lane, unsigned wave) {
  const __amdgpu_buffer_rsrc_t rA = __builtin_amdgcn_make_buffer_rsrc((void*)wq, 0, DM * NH * DH * 2, 0x00020000);
  unsigned vA[6], rr[3], cc[3];
#pragma unroll
  for (int m = 0; m < 3; ++m) {
    const unsigned u = 64 * m + lane, r = (u * 2731) >> 16, c = u - 24 * r;
    rr[m] = r; cc[m] = c;
    vA[m] = r * 12288 + ((c ^ (r & 2)) << 4);
    vA[m + 3] = (vA[m] ^ 64) + 8 * 12288;
  }
  unsigned kcx = G.c0;
  auto issueA = [&](unsigned n) {
    const unsigned slot = (n & 3) * SLOT + dw * 12288, so = (kcx * 64 + dw * 32) * 12288 + G.h * 384;
#pragma unroll
    for (int p = 0; p < 12; ++p) dma16(rA, slot + p * 1024, vA[p % 6], so + (p / 6) * 16 * 12288);
    kcx = kcx == 9 ? 0u : kcx + 1;
  };
#pragma nounroll
  for (unsigned n = 0; n < 3; ++n) issueA(n);
  const __amdgpu_buffer_rsrc_t rK = __builtin_amdgcn_make_buffer_rsrc((void*)kc, 0, NB * NH * SK * DH * 2, 0x00020000);
  const __amdgpu_buffer_rsrc_t rB = __builtin_amdgcn_make_buffer_rsrc((void*)wo, 0, DM * NH * DH * 2, 0x00020000);
  unsigned vK[6], vB[5];
#pragma unroll
  for (int m = 0; m < 3; ++m) {
    vK[m] = rr[m] * 384 + ((cc[m] ^ (rr[m] >> 1)) << 4);
    vK[m + 3] = (vK[m] ^ 64) + 8 * 384;
  }
#pragma unroll
  for (int m = 0; m < 5; ++m) {
    const unsigned u = 64 * m + lane, r = (u * 3277) >> 16, c = u - 20 * r;
    vB[m] = r * 1280 + ((c ^ ((r >> 2) & 2)) << 4);
  }
  unsigned row = 192 * G.j;
  const unsigned sb = (768 * G.g + dw * 32) * 1280 + 320 * G.j;
  auto issueB = [&](unsigned n) {
    const unsigned slot = (n & 3) * SLOT + dw * 10240, so = sb + row * 1280;
#pragma unroll
    for (int p = 0; p < 10; ++p) dma16(rB, slot + p * 1024, vB[p % 5], so + (p / 5) * 16 * 1280);
    row = row == 704 ? 0u : row + 64;
  };
  WAITVM(24); BAR();
  STAMP(1);
#pragma nounroll
  for (unsigned t = 0; t < 7; ++t) { issueA(t + 3); STAMPD(16 + 4 * t); WAITVM(24); STAMPD(17 + 4 * t); BAR(); }
  { const unsigned slot = (10 & 3) * SLOT + dw * 12288, so = ((G.b * 32 + G.h) * 64 + dw * 32) * 384;
#pragma unroll
    for (int p = 0; p < 12; ++p) dma16(rK, slot + p * 1024, vK[p % 6], so + (p / 6) * 16 * 384); }
  WAITVM(24); BAR();
#pragma nounroll
  for (unsigned t = 8; t < 20; ++t) {
    if (t == 10) BAR();
    issueB(t + 3);
    STAMPD(16 + 4 * t);
    if (t == 8) WAITVM(22); else WAITVM(20);
    STAMPD(17 + 4 * t);
    BAR();
  }
  WAITVM(10); BAR();
  WAITVM(0); BAR();
  BAR();
  STAMP(3);
}
DI void dma_wave_a(const Geo& G, const void* x, const void* vc, unsigned lane, unsigned wave) {
  const __amdgpu_buffer_rsrc_t rA = __builtin_amdgcn_make_buffer_rsrc((void*)x, 0, NB * SQ * DM * 2, 0x00020000);
  const unsigned r = lane >> 3, c = lane & 7, cs = (c ^ (r >> 1)) << 4;
  const unsigned vA0 = r * 1280 + cs, vA1 = r * 1280 + (cs ^ 64);
  unsigned kcx = G.c0;
  auto issueA = [&](unsigned n) {
    const unsigned slot = (n & 3) * SLOT + AUX, so = G.b * 64 * 1280 + kcx * 128;
#pragma unroll
    for (int p = 0; p < 8; ++p) dma16(rA, slot + p * 1024, (p & 1) ? vA1 : vA0, so + p * 8 * 1280);
    kcx = kcx == 9 ? 0u : kcx + 1;
  };
#pragma nounroll
  for (unsigned n = 0; n < 3; ++n) issueA(n);
  const __amdgpu_buffer_rsrc_t rB = __builtin_amdgcn_make_buffer_rsrc((void*)vc, 0, NB * NH * SK * DH * 2, 0x00020000);
  const unsigned vB0 = r * 384 + cs, vB1 = r * 384 + (cs ^ 64);
  unsigned hh = G.j, dc = 0;
  auto issueB = [&](unsigned n) {
    const unsigned slot = (n & 3) * SLOT + AUX, so = (G.b * 32 + 4 * G.g + hh) * 24576 + dc * 128;
#pragma unroll
    for (int p = 0; p < 8; ++p) dma16(rB, slot + p * 1024, (p & 1) ? vB1 : vB0, so + p * 8 * 384);
    hh = dc == 2 ? (hh + 1) & 3 : hh;
    dc = dc == 2 ? 0u : dc + 1;
  };
  WAITVM(16); BAR();
#pragma nounroll
  for (unsigned t = 0; t < 7; ++t) { issueA(t + 3); WAITVM(16); BAR(); }
  WAITVM(8); BAR();
#pragma nounroll
  for (unsigned t = 8; t < 20; ++t) {
    if (t == 10) BAR();
    issueB(t + 3);
    if (t == 8) WAITVM(8); else WAITVM(16);
    BAR();
  }
  WAITVM(8); BAR();
  WAITVM(0); BAR();
  BAR();
  STAMP(3);
}

DI void reduce_tile(const Geo& G, unsigned dt, u64 tagR, char* ws, void* out, unsigned lane, bool upd) {
  const unsigned nt = dt >> 1, ip = dt & 1;
  const unsigned pw = nt < 3 ? 0u : nt < 6 ? 1u : nt < 8 ? 2u : 3u;
  const char* fl = ws + WS_FR + (size_t)((((G.b * 4 + G.j) * 8 + (lane & 7)) * 4 + pw) * 128);
#pragma nounroll
  for (unsigned sp = 0; sp < SPIN_CAP; ++sp) {
    u64 v = ld_sc1_64(fl);
    if (__all(v == tagR)) break;
  }
  CBAR();
  __amdgpu_buffer_rsrc_t rR = __builtin_amdgcn_make_buffer_rsrc((void*)(ws + WS_R), 0, 256 * 20480, 0x00020000);
  const unsigned base = ((G.b * 4 + G.j) * 8) * 20480 + dt * 1024;
  u32x4 v[8];
#pragma unroll
  for (int s = 0; s < 8; ++s) v[s] = __builtin_amdgcn_raw_buffer_load_b128(rR, lane * 16, base + s * 20480, LD_AUX);
  float a[8];
#pragma unroll
  for (int k = 0; k < 4; ++k) { a[2 * k] = __uint_as_float(v[0][k] << 16); a[2 * k + 1] = __uint_as_float(v[0][k] & 0xffff0000u); }
#pragma unroll
  for (int s = 1; s < 8; ++s)
#pragma unroll
    for (int k = 0; k < 4; ++k) { a[2 * k] += __uint_as_float(v[s][k] << 16); a[2 * k + 1] += __uint_as_float(v[s][k] & 0xffff0000u); }
  __amdgpu_buffer_rsrc_t rO = __builtin_amdgcn_make_buffer_rsrc(out, 0, NB * SQ * DM * 2, 0x00020000);
  const unsigned row = G.b * 64 + 32 * ip + (lane & 15), col = 160 * G.j + 16 * nt + 4 * (lane >> 4);
  u32x2 o0 = {cvtpk(a[0], a[1]), cvtpk(a[2], a[3])}, o1 = {cvtpk(a[4], a[5]), cvtpk(a[6], a[7])};
  __builtin_amdgcn_raw_buffer_store_b64(o0, rO, (row * DM + col) * 2, 0, 0);
  __builtin_amdgcn_raw_buffer_store_b64(o1, rO, ((row + 16) * DM + col) * 2, 0, 0);
  if (upd && lane == 0) st_sc1_64(ws + WS_ER + (G.b * 4 + G.j) * 128, tagR);
}

struct Frag { u32x4 f[7]; };
#define F0_OPS(F) "+{v[200:203]}"(F.f[0]), "+{v[204:207]}"(F.f[1]), "+{v[208:211]}"(F.f[2]), "+{v[212:215]}"(F.f[3]), "+{v[216:219]}"(F.f[4]), "+{v[220:223]}"(F.f[5]), "+{v[224:227]}"(F.f[6])
#define F1_CLOB "v228", "v229", "v230", "v231", "v232", "v233", "v234", "v235", "v236", "v237", "v238", "v239", "v240", "v241", "v242", "v243", "v244", "v245", "v246", "v247", "v248", "v249", "v250", "v251", "v252", "v253", "v254", "v255"
#define RUN_CHUNK(TXT, acc, F, ad, delta) asm volatile(TXT \
    : "+v"(acc[0]), "+v"(acc[1]), "+v"(acc[2]), "+v"(acc[3]), "+v"(acc[4]), "+v"(acc[5]), "+v"(acc[6]), "+v"(acc[7]), "+v"(acc[8]), "+v"(acc[9]), "+v"(acc[10]), "+v"(acc[11]), \
      F0_OPS(F), "+v"(ad[0]), "+v"(ad[1]), "+v"(ad[2]), "+v"(ad[3]), "+v"(ad[4]) : "s"(delta) : F1_CLOB, "memory")
#define RUN_CHUNK0(TXT, acc, F, ad, delta) asm volatile(TXT \
    : "=&v"(acc[0]), "=&v"(acc[1]), "=&v"(acc[2]), "=&v"(acc[3]), "=&v"(acc[4]), "=&v"(acc[5]), "=&v"(acc[6]), "=&v"(acc[7]), "=&v"(acc[8]), "=&v"(acc[9]), "=&v"(acc[10]), "=&v"(acc[11]), \
      F0_OPS(F), "+v"(ad[0]), "+v"(ad[1]), "+v"(ad[2]), "+v"(ad[3]), "+v"(ad[4]) : "s"(delta) : F1_CLOB, "memory")
#define RUN_LOAD(TXT, F, ad) asm volatile(TXT : F0_OPS(F) : "v"(ad[0]), "v"(ad[1]), "v"(ad[2]), "v"(ad[3]) : "memory")
DI unsigned slot_delta(unsigned c) { return (c & 3) == 3 ? (unsigned)(-3 * (int)SLOT) : SLOT; }

__global__ void __launch_bounds__(512, 2)
mha_fused(const void* __restrict__ x, const void* __restrict__ kc, const void* __restrict__ vc, const void* __restrict__ wq,
          const void* __restrict__ wo, void* __restrict__ out, char* __restrict__ ws) {
  const unsigned tid = threadIdx.x, lane = tid & 63, wave = rfl(tid >> 6);
  Geo G;
  { const unsigned bid = blockIdx.x; G.g = bid & 7; const unsigned idx = bid >> 3; G.j = idx & 3; G.b = idx >> 2; G.h = 4 * G.g + G.j;
    const unsigned fb = (G.b * 10) >> 3, oj = (G.j * 10 + 2) >> 2;
    unsigned c0 = fb + oj; if (c0 >= 10) c0 -= 10; G.c0 = ROTATE ? c0 : 0u; }
  u64 pc0 = 0;
  if (CODEPF) asm volatile("s_getpc_b64 %0" : "=s"(pc0));
  STAMP_BEGIN();
  if (wave >= 4 && wave < 7) {
    if (wave < 6) dma_wave_w(G, wave - 4, kc, wq, wo, lane, wave);
    else dma_wave_a(G, x, vc, lane, wave);
    const u64 tagR = lds_u64(MISC + L_TAGR);
    const unsigned dt = G.g + 8 * (wave - 4);
    if (dt < 20) reduce_tile(G, dt, tagR, ws, out, lane, wave == 4 && G.g == 0);
    STAMP(5);
  } else if (wave == 7) {
    const u64 eP = ld_sc1_64(ws + WS_EP + (G.b * 8 + G.g) * 128);
    const u64 eR = ld_sc1_64(ws + WS_ER + (G.b * 4 + G.j) * 128);
    unsigned tch[4] = {0, 0, 0, 0};
    if (PRETOUCH) {
      const char* pa;
      const unsigned l = lane & 15;
      if (l < 4) pa = (const char*)wq + (size_t)l * 2097152 + G.h * 384;
      else if (l == 4) pa = (const char*)kc + (size_t)(G.b * 32 + G.h) * 24576;
      else if (l == 5) pa = (const char*)vc + (size_t)(G.b * 32 + 4 * G.g) * 24576;
      else if (l == 6) pa = (const char*)vc + (size_t)(G.b * 32 + 4 * G.g + 4) * 24576 - 128;
      else if (l == 7) pa = (const char*)wo + (size_t)(768 * G.g) * 1280;
      else if (l == 8) pa = (const char*)wo + (size_t)(768 * G.g + 767) * 1280;
      else if (l == 9) pa = ws + WS_P + (size_t)(G.b * 32 + 4 * G.g) * 8192;
      else if (l == 10) pa = ws + WS_R + (size_t)((G.b * 4 + G.j) * 8) * 20480;
      else if (l == 11) pa = ws + WS_R + (size_t)((G.b * 4 + G.j) * 8 + 8) * 20480 - 128;
      else if (l == 12) pa = (const char*)out + (size_t)G.b * 64 * 1280;
      else if (l == 13) pa = ws + WS_FP + (size_t)(G.b * 32 + 4 * G.g) * 512;
      else if (l == 14) pa = ws + WS_FR + (size_t)(G.b * 4 + G.j) * 4096;
      else pa = (const char*)x + (size_t)G.b * 64 * 1280;
      asm volatile("global_load_dword %0, %1, off" : "=v"(tch[0]) : "v"(pa) : "memory");
    }
    if (CODEPF) {
#pragma unroll
      for (int i = 0; i < 3; ++i) { const unsigned o = (i * 64 + lane) * 128; const char* pa = (const char*)pc0 + (o < CODEPF ? o : 0u);
        asm volatile("global_load_dword %0, %1, off" : "=v"(tch[1 + i]) : "v"(pa) : "memory"); }
    }
    BAR(); BAR(); BAR(); BAR(); BAR();
    asm volatile("s_waitcnt vmcnt(0)" : "+v"(tch[0]), "+v"(tch[1]), "+v"(tch[2]), "+v"(tch[3]) :: "memory");
    const u64 tagP = next_tag(eP), tagR = next_tag(eR);
    if (lane == 0) { lds_st32(MISC + L_TAGP, (unsigned)tagP); lds_st32(MISC + L_TAGP + 4, (unsigned)(tagP >> 32));
                     lds_st32(MISC + L_TAGR, (unsigned)tagR); lds_st32(MISC + L_TAGR + 4, (unsigned)(tagR >> 32)); }
    BAR_L(); BAR(); BAR(); BAR(); BAR(); BAR();
    BAR(); BAR(); BAR(); BAR();
    STAMP(1);
    const __amdgpu_buffer_rsrc_t rP = __builtin_amdgcn_make_buffer_rsrc((void*)(ws + WS_P), 0, 256 * 8192, 0x00020000);
    const unsigned pl = opaque(PBUF + lane * 16);
    const char* fl = ws + WS_FP + (size_t)((G.b * 32 + 4 * G.g) * 4 + (lane & 15)) * 128;
    unsigned s = 13;
    {
      u64 pv;
      asm volatile("global_load_dwordx2 %0, %1, off sc1" : "=v"(pv) : "v"(fl) : "memory");
#pragma nounroll
      for (unsigned sp = 0; sp < SPIN_CAP; ++sp) {
        if (s == 20 || vm_idle_soon()) {
          asm volatile("s_waitcnt vmcnt(0)" : "+v"(pv) :: "memory");
          if (__all(pv == tagP)) break;
          asm volatile("global_load_dwordx2 %0, %1, off sc1" : "=v"(pv) : "v"(fl) : "memory");
        }
        if (s < 20) { BAR(); ++s; }
      }
      asm volatile("s_waitcnt vmcnt(0)" : "+v"(pv) :: "memory");
    }
    STAMP(2);
    u32x4 f[3][8];
#pragma unroll
    for (int u = 0; u < 3; ++u) {
      const unsigned hh = (G.j + u + 1) & 3;
#pragma unroll
      for (int i = 0; i < 8; ++i) f[u][i] = __builtin_amdgcn_raw_buffer_load_b128(rP, lane * 16, (G.b * 32 + 4 * G.g + hh) * 8192 + i * 1024, LD_AUX);
    }
    if (G.j == 0 && lane == 0) st_sc1_64(ws + WS_EP + (G.b * 8 + G.g) * 128, tagP);
#pragma nounroll
    while (s < 20 && !vm_idle_soon()) { BAR(); ++s; }
    STAMP(3);
#pragma unroll
    for (int i = 0; i < 8; ++i) *(LAS u32x4*)(uintptr_t)(pl + (i >> 1) * 6144 + 2048 + (i & 1) * 1024) = f[0][i];
    if (s < 20) { BAR_L(); ++s; }
#pragma unroll
    for (int i = 0; i < 8; ++i) *(LAS u32x4*)(uintptr_t)(pl + (i >> 1) * 6144 + 4096 + (i & 1) * 1024) = f[1][i];
    if (s < 20) { BAR_L(); ++s; }
#pragma nounroll
    while (s < 15) { BAR(); ++s; }
#pragma unroll
    for (int i = 0; i < 8; ++i) *(LAS u32x4*)(uintptr_t)(pl + (i >> 1) * 6144 + (i & 1) * 1024) = f[2][i];
    STAMP(4);
#pragma nounroll
    while (s <= 20) { BAR_L(); ++s; }
    BAR(); BAR();
    STAMP(6);
  } else {
    const unsigned i16 = lane & 15, g4 = lane >> 4, q = i16 >> 2, p = lane & 3;
    const unsigned a0 = AUX + i16 * 128 + ((g4 ^ (i16 >> 1)) << 4);
    Frag F0;
#pragma unroll
    for (int i = 0; i < 7; ++i) F0.f[i] = (u32x4){0, 0, 0, 0};
    unsigned ad[5];
    f32x4 acc[12];
#pragma unroll
    for (int t = 0; t < 12; ++t) acc[t] = (f32x4){0.f, 0.f, 0.f, 0.f};
    { const unsigned phi = ((q >> 1) & 1) | ((g4 & 1) << 1);
#pragma unroll
      for (int e = 0; e < 3; ++e) ad[e] = (8 * g4 + q) * 384 + 32 * ((3 * wave + e) ^ phi) + 16 * (p >> 1) + 8 * (p & 1);
      ad[3] = a0; ad[4] = a0 ^ 64; }
    asm volatile("" : "+v"(acc[0]), "+v"(acc[1]), "+v"(acc[2]), "+v"(acc[3]), "+v"(acc[4]), "+v"(acc[5]), "+v"(acc[6]), "+v"(acc[7]), "+v"(acc[8]), "+v"(acc[9]), "+v"(acc[10]), "+v"(acc[11]),
                      "+v"(ad[0]), "+v"(ad[1]), "+v"(ad[2]), "+v"(ad[3]), "+v"(ad[4]));
    BAR();
    STAMP(1);
    RUN_LOAD(ASM_LOAD_A, F0, ad);
#pragma nounroll
    for (unsigned c = 0; c < 10; ++c) { const unsigned dl = slot_delta(c); RUN_CHUNK(ASM_CHUNK_A, acc, F0, ad, dl); }
    STAMP(2);
    {
      const unsigned qw = opaque(PBUF + ((g4 >> 1) * 16 + i16) * 16 + (g4 & 1) * 8);
#pragma unroll
      for (int e = 0; e < 3; ++e) {
        const unsigned tau = 3 * wave + e;
        const unsigned qe = qw + (tau >> 1) * 1024 + (tau & 1) * 512;
#pragma unroll
        for (int t = 0; t < 4; ++t) { u32x2 v = {cvtpk(acc[4 * e + t][0], acc[4 * e + t][1]), cvtpk(acc[4 * e + t][2], acc[4 * e + t][3])};
          *(LAS u32x2*)(uintptr_t)(qe + t * 6144) = v; }
      }
    }
    BAR_L();
    STAMP(3);
    f32x4 st[4];
    {
      const unsigned qr = opaque(PBUF + wave * 6144 + lane * 16);
      bf16x8 qf[6];
#pragma unroll
      for (int s = 0; s < 6; ++s) qf[s] = lds_b128(qr + s * 1024);
      const unsigned slot = (10 & 3) * SLOT, fk = i16 >> 1;
      unsigned ka[2];
#pragma unroll
      for (int so = 0; so < 2; ++so) ka[so] = opaque(slot + i16 * 384 + (((4 * so + g4) ^ fk) << 4));
      bf16x8 kf[6][4];
#pragma unroll
      for (int s = 0; s < 6; ++s)
#pragma unroll
        for (int kt = 0; kt < 4; ++kt) kf[s][kt] = lds_b128(ka[s & 1] + kt * 16 * 384 + 128 * (s >> 1));
      BAR_L();
      SB0();
#pragma unroll
      for (int kt = 0; kt < 4; ++kt) st[kt] = (f32x4){0.f, 0.f, 0.f, 0.f};
#pragma unroll
      for (int s = 0; s < 6; ++s)
#pragma unroll
        for (int kt = 0; kt < 4; ++kt) st[kt] = MFMA(kf[s][kt], qf[s], st[kt]);
    }
    SB0();
    const unsigned ntb = wave == 3 ? 7u : 3u * wave;
    { const unsigned slot = (11 & 3) * SLOT;
#pragma unroll
      for (int e = 0; e < 3; ++e) ad[e] = slot + (8 * g4 + q) * 320 + 32 * ((ntb + e) ^ (g4 & 1)) + 16 * (p >> 1) + 8 * (p & 1);
      ad[3] = slot + a0; ad[4] = slot + (a0 ^ 64); }
    RUN_LOAD(ASM_LOAD_B, F0, ad);
    asm volatile("s_nop 7" : "+v"(st[0]), "+v"(st[1]), "+v"(st[2]), "+v"(st[3]));
    const __amdgpu_buffer_rsrc_t rP = __builtin_amdgcn_make_buffer_rsrc((void*)(ws + WS_P), 0, 256 * 8192, 0x00020000);
    {
      float m = __builtin_amdgcn_fmed3f(st[0][0], st[0][1], __builtin_inff());
      m = max3f(m, st[0][2], st[0][3]);
#pragma unroll
      for (int kt = 1; kt < 4; ++kt) { m = max3f(m, st[kt][0], st[kt][1]); m = max3f(m, st[kt][2], st[kt][3]); }
      { auto r1 = __builtin_amdgcn_permlane16_swap(__float_as_uint(m), __float_as_uint(m), false, false);
        m = max3f(__uint_as_float(r1[0]), __uint_as_float(r1[1]), __uint_as_float(r1[1]));
        auto r2 = __builtin_amdgcn_permlane32_swap(__float_as_uint(m), __float_as_uint(m), false, false);
        m = max3f(__uint_as_float(r2[0]), __uint_as_float(r2[1]), __uint_as_float(r2[1])); }
      constexpr float SC = 0.07216878364870322f * 1.4426950408889634f;
      const float ml = m * SC;
      float sum = 0.f;
#pragma unroll
      for (int kt = 0; kt < 4; ++kt)
#pragma unroll
        for (int r = 0; r < 4; ++r) { const float e = __builtin_amdgcn_exp2f(__builtin_fmaf(st[kt][r], SC, -ml)); st[kt][r] = e; sum += e; }
      { auto r1 = __builtin_amdgcn_permlane16_swap(__float_as_uint(sum), __float_as_uint(sum), false, false);
        sum = __uint_as_float(r1[0]) + __uint_as_float(r1[1]);
        auto r2 = __builtin_amdgcn_permlane32_swap(__float_as_uint(sum), __float_as_uint(sum), false, false);
        sum = __uint_as_float(r2[0]) + __uint_as_float(r2[1]); }
      const float inv = __builtin_amdgcn_rcpf(sum);
#pragma unroll
      for (int kt = 0; kt < 4; ++kt) st[kt] *= inv;
      const unsigned pw = opaque(PBUF + wave * 6144 + lane * 16);
#pragma unroll
      for (int s2 = 0; s2 < 2; ++s2) {
        const u32x4 pf = __builtin_bit_cast(u32x4, pack8(st[2 * s2], st[2 * s2 + 1]));
        __builtin_amdgcn_raw_buffer_store_b128(pf, rP, lane * 16, (G.b * 32 + G.h) * 8192 + (2 * wave + s2) * 1024, ST_AUX);
        *(LAS u32x4*)(uintptr_t)(pw + s2 * 1024) = pf;
      }
    }
    STAMP(4);
    f32x4 oa[4][3];
#pragma unroll
    for (int it = 0; it < 4; ++it)
#pragma unroll
      for (int e = 0; e < 3; ++e) oa[it][e] = (f32x4){0.f, 0.f, 0.f, 0.f};
    auto pvw = [&](unsigned buf, const bf16x8 (&pk)[2][3]) {
      const unsigned pb = opaque(PBUF + buf * 2048 + lane * 16);
      bf16x8 pf[4][2];
#pragma unroll
      for (int s2 = 0; s2 < 2; ++s2)
#pragma unroll
        for (int it = 0; it < 4; ++it) pf[it][s2] = lds_b128(pb + it * 6144 + s2 * 1024);
      SB0();
#pragma unroll
      for (int s2 = 0; s2 < 2; ++s2)
#pragma unroll
        for (int it = 0; it < 4; ++it)
#pragma unroll
          for (int e = 0; e < 3; ++e) oa[it][e] = MFMA(pk[s2][e], pf[it][s2], oa[it][e]);
      SB0();
    };
    f32x4 vw[12];
    auto pack_vw = [&](bf16x8 (&pk)[2][3]) {
#pragma unroll
      for (int s2 = 0; s2 < 2; ++s2)
#pragma unroll
        for (int e = 0; e < 3; ++e) pk[s2][e] = pack8(vw[3 * (2 * s2) + e], vw[3 * (2 * s2 + 1) + e]);
    };
    auto head_first = [&](unsigned c) { const unsigned dl = slot_delta(c); RUN_CHUNK0(ASM_CHUNK_B0, vw, F0, ad, dl); };
    auto head_next = [&](unsigned c) { const unsigned dl = slot_delta(c); RUN_CHUNK(ASM_CHUNK_B, vw, F0, ad, dl); };
    bf16x8 pk1[2][3], pk2[2][3];
    head_first(11);
    head_next(12);
    {
      const u64 tagP = lds_u64(MISC + L_TAGP);
      WAITVM(0);
      if (lane == 0) st_sc1_64(ws + WS_FP + (size_t)((G.b * 32 + G.h) * 4 + wave) * 128, tagP); }
    head_next(13);
    STAMP(5);
    pack_vw(pk1); pvw(0, pk1);
    head_first(14); head_next(15); head_next(16);
    STAMP(6);
    pack_vw(pk1);
    head_first(17); head_next(18); head_next(19);
    STAMP(7);
    pack_vw(pk2);
    head_first(20);
    pvw(1, pk1);
    head_next(21);
    pvw(2, pk2);
    head_next(22);
    STAMP(8);
    pack_vw(pk1); pvw(0, pk1);
    STAMP(9);
    const u64 tagR = lds_u64(MISC + L_TAGR);
    __amdgpu_buffer_rsrc_t rR = __builtin_amdgcn_make_buffer_rsrc((void*)(ws + WS_R), 0, 256 * 20480, 0x00020000);
    const unsigned base = ((G.b * 4 + G.j) * 8 + G.g) * 20480;
#pragma unroll
    for (int e = 0; e < 3; ++e) {
      const bool keep = wave < 2 || (wave == 2 ? e < 2 : e > 0);
      if (keep) {
#pragma unroll
        for (int ip = 0; ip < 2; ++ip)
          __builtin_amdgcn_raw_buffer_store_b128(__builtin_bit_cast(u32x4, pack8(oa[2 * ip][e], oa[2 * ip + 1][e])), rR, lane * 16, base + (2 * (ntb + e) + ip) * 1024, ST_AUX);
      }
    }
    WAITVM(0);
    if (lane == 0) st_sc1_64(ws + WS_FR + (size_t)((((G.b * 4 + G.j) * 8 + G.g) * 4 + wave) * 128), tagR);
    STAMP(10);
  }
  STAMP_END();
}

extern "C" void kernel_launch(void* const* d_in, const int* in_sizes, int n_in, void* d_out, int out_size, void* d_ws, size_t ws_size,
                              hipStream_t stream) {
  (void)in_sizes; (void)n_in; (void)out_size; (void)ws_size;
  (void)hipFuncSetAttribute((const void*)mha_fused, hipFuncAttributeMaxDynamicSharedMemorySize, LDS_BYTES);
  mha_fused<<<256, 512, LDS_BYTES, stream>>>(d_in[0], d_in[1], d_in[2], d_in[3], d_in[4], d_out, (char*)d_ws);
}
```

```cpp
#include <hip/hip_runtime.h>
#include <hip/hip_bf16.h>
#include <stdint.h>

#define LAS __attribute__((address_space(3)))
#define GAS __attribute__((address_space(1)))
#define DI __device__ __forceinline__
typedef short s16x4 __attribute__((ext_vector_type(4)));
typedef __bf16 bf16x2 __attribute__((ext_vector_type(2)));
typedef __bf16 bf16x8 __attribute__((ext_vector_type(8)));
typedef float f32x2 __attribute__((ext_vector_type(2)));
typedef float f32x4 __attribute__((ext_vector_type(4)));
typedef unsigned u32x2 __attribute__((ext_vector_type(2)));
typedef unsigned u32x4 __attribute__((ext_vector_type(4)));
typedef unsigned long long u64;

constexpr int NB = 8, SQ = 64, DM = 640, NH = 32, SK = 64, DH = 192;
constexpr int NCH = 22;
constexpr unsigned SLOT = 32768, AUX = 24576;
constexpr unsigned PBUF = 131072, MISC = PBUF + 3 * 8192, LDS_USED = MISC + 256;
constexpr unsigned L_TAGP = 0, L_TAGR = 8;
constexpr unsigned WS_EP = 0;
constexpr unsigned WS_ER = 8192;
constexpr unsigned WS_FP = 16384;
constexpr unsigned WS_FR = WS_FP + 131072;
constexpr unsigned WS_P = 524288;
constexpr unsigned WS_R = WS_P + 256 * 8192;
constexpr unsigned WS_DBG = 16u << 20;
constexpr unsigned SPIN_CAP = 1u << 16;

#define STAMPS 0
#define ROTATE 1
#define ROTB 1
#define PF 1
#define PRETOUCH 0
#define CODEPF 0
#define IN_AUX 0
#define ST_AUX 16
#define LD_AUX 16
constexpr unsigned LDS_BYTES = LDS_USED + (STAMPS ? 4096 : 0);

DI unsigned rfl(unsigned x) { return __builtin_amdgcn_readfirstlane(x); }
DI unsigned cvtpk(float lo, float hi) { f32x2 v = {lo, hi}; bf16x2 b = __builtin_convertvector(v, bf16x2); return __builtin_bit_cast(unsigned, b); }
DI bf16x8 pack8(const f32x4& a, const f32x4& b) {
  u32x4 p = {cvtpk(a[0], a[1]), cvtpk(a[2], a[3]), cvtpk(b[0], b[1]), cvtpk(b[2], b[3])};
  return __builtin_bit_cast(bf16x8, p);
}
#define MFMA(a, b, c) __builtin_amdgcn_mfma_f32_16x16x32_bf16((a), (b), (c), 0, 0, 0)
DI bf16x8 lds_b128(unsigned addr) { return *(const LAS bf16x8*)(uintptr_t)addr; }
DI unsigned lds_u32(unsigned addr) { return *(const volatile LAS unsigned*)(uintptr_t)addr; }
DI void lds_st32(unsigned addr, unsigned v) { *(volatile LAS unsigned*)(uintptr_t)addr = v; }
DI u64 lds_u64(unsigned addr) { return (u64)lds_u32(addr) | ((u64)lds_u32(addr + 4) << 32); }
#define CBAR() asm volatile("" ::: "memory")
#define SB0() __builtin_amdgcn_sched_barrier(0)
#define BAR() asm volatile("s_barrier" ::: "memory")
#define BAR_L() asm volatile("s_waitcnt lgkmcnt(0)\n\ts_barrier" ::: "memory")
#define WAITVM(n) asm volatile("s_waitcnt vmcnt(" #n ")" ::: "memory")
DI void dma16(__amdgpu_buffer_rsrc_t r, unsigned lds, unsigned voff, unsigned soff) {
  __builtin_amdgcn_raw_ptr_buffer_load_lds(r, (LAS void*)(uintptr_t)lds, 16, voff, soff, 0, IN_AUX);
}
DI unsigned opaque(unsigned x) { asm volatile("" : "+v"(x)); return x; }
DI float max3f(float a, float b, float c) { float r; asm("v_max3_f32 %0, %1, %2, %3" : "=v"(r) : "v"(a), "v"(b), "v"(c)); return r; }
DI u64 next_tag(u64 e) { return e * 6364136223846793005ull + 1442695040888963407ull; }
DI u64 ld_sc1_64(const void* p) { return __hip_atomic_load((const GAS u64*)p, __ATOMIC_RELAXED, __HIP_MEMORY_SCOPE_AGENT); }
DI void st_sc1_64(void* p, u64 v) { __hip_atomic_store((GAS u64*)p, v, __ATOMIC_RELAXED, __HIP_MEMORY_SCOPE_AGENT); }
DI bool vm_idle() { unsigned r; asm volatile("s_getreg_b32 %0, hwreg(HW_REG_IB_STS)" : "=s"(r) :: "memory"); return (r & 0x00C0000Fu) == 0; }
DI bool vm_idle_soon() {
#pragma nounroll
  for (int k = 0; k < 4; ++k) { if (vm_idle()) return true; __builtin_amdgcn_s_sleep(1); }
  return false;
}
#define STAMP(i) do {} while (0)
#define STAMP_BEGIN() do {} while (0)
#define STAMP_END() do {} while (0)
#define STAMPD(i) do {} while (0)

#define ASM_CHUNK_A \
  "s_nop 1\n\t" \
  "s_waitcnt lgkmcnt(0)\n\t" \
  "v_mfma_f32_16x16x32_bf16 %0, v[200:203], v[212:215], %0\n\t" \
  "ds_read_b64_tr_b16 v[228:229], %19 offset:12288\n\t" \
  "ds_read_b64_tr_b16 v[230:231], %19 offset:13824\n\t" \
  "v_mfma_f32_16x16x32_bf16 %1, v[200:203], v[216:219], %1\n\t" \
  "ds_read_b128 v[240:243], %23 offset:0\n\t" \
  "ds_read_b128 v[244:247], %23 offset:2048\n\t" \
  "v_mfma_f32_16x16x32_bf16 %2, v[200:203], v[220:223], %2\n\t" \
  "ds_read_b128 v[248:251], %23 offset:4096\n\t" \
  "ds_read_b128 v[252:255], %23 offset:6144\n\t" \
  "v_mfma_f32_16x16x32_bf16 %3, v[200:203], v[224:227], %3\n\t" \
  "ds_read_b64_tr_b16 v[232:233], %20 offset:12288\n\t" \
  "ds_read_b64_tr_b16 v[234:235], %20 offset:13824\n\t" \
  "v_mfma_f32_16x16x32_bf16 %4, v[204:207], v[212:215], %4\n\t" \
  "ds_read_b64_tr_b16 v[236:237], %21 offset:12288\n\t" \
  "ds_read_b64_tr_b16 v[238:239], %21 offset:13824\n\t" \
  "v_mfma_f32_16x16x32_bf16 %5, v[204:207], v[216:219], %5\n\t" \
  "v_add_u32 %19, %24, %19\n\t" \
  "v_mfma_f32_16x16x32_bf16 %6, v[204:207], v[220:223], %6\n\t" \
  "v_add_u32 %20, %24, %20\n\t" \
  "v_mfma_f32_16x16x32_bf16 %7, v[204:207], v[224:227], %7\n\t" \
  "v_add_u32 %21, %24, %21\n\t" \
  "v_mfma_f32_16x16x32_bf16 %8, v[208:211], v[212:215], %8\n\t" \
  "v_add_u32 %22, %24, %22\n\t" \
  "v_mfma_f32_16x16x32_bf16 %9, v[208:211], v[216:219], %9\n\t" \
  "v_add_u32 %23, %24, %23\n\t" \
  "v_mfma_f32_16x16x32_bf16 %10, v[208:211], v[220:223], %10\n\t" \
  "v_mfma_f32_16x16x32_bf16 %11, v[208:211], v[224:227], %11\n\t" \
  "s_waitcnt lgkmcnt(0)\n\t" \
  "s_barrier\n\t" \
  "v_mfma_f32_16x16x32_bf16 %0, v[228:231], v[240:243], %0\n\t" \
  "ds_read_b64_tr_b16 v[200:201], %19 offset:0\n\t" \
  "ds_read_b64_tr_b16 v[202:203], %19 offset:1536\n\t" \
  "v_mfma_f32_16x16x32_bf16 %1, v[228:231], v[244:247], %1\n\t" \
  "ds_read_b128 v[212:215], %22 offset:0\n\t" \
  "ds_read_b128 v[216:219], %22 offset:2048\n\t" \
  "v_mfma_f32_16x16x32_bf16 %2, v[228:231], v[248:251], %2\n\t" \
  "ds_read_b128 v[220:223], %22 offset:4096\n\t" \
  "ds_read_b128 v[224:227], %22 offset:6144\n\t" \
  "v_mfma_f32_16x16x32_bf16 %3, v[228:231], v[252:255], %3\n\t" \
  "ds_read_b64_tr_b16 v[204:205], %20 offset:0\n\t" \
  "ds_read_b64_tr_b16 v[206:207], %20 offset:1536\n\t" \
  "v_mfma_f32_16x16x32_bf16 %4, v[232:235], v[240:243], %4\n\t" \
  "ds_read_b64_tr_b16 v[208:209], %21 offset:0\n\t" \
  "ds_read_b64_tr_b16 v[210:211], %21 offset:1536\n\t" \
  "v_mfma_f32_16x16x32_bf16 %5, v[232:235], v[244:247], %5\n\t" \
  "v_mfma_f32_16x16x32_bf16 %6, v[232:235], v[248:251], %6\n\t" \
  "v_mfma_f32_16x16x32_bf16 %7, v[232:235], v[252:255], %7\n\t" \
  "v_mfma_f32_16x16x32_bf16 %8, v[236:239], v[240:243], %8\n\t" \
  "v_mfma_f32_16x16x32_bf16 %9, v[236:239], v[244:247], %9\n\t" \
  "v_mfma_f32_16x16x32_bf16 %10, v[236:239], v[248:251], %10\n\t" \
  "v_mfma_f32_16x16x32_bf16 %11, v[236:239], v[252:255], %11\n\t" \
  "s_waitcnt lgkmcnt(0)\n\t" \
  "s_nop 7\n\t"

#define ASM_CHUNK_B \
  "s_nop 1\n\t" \
  "s_waitcnt lgkmcnt(0)\n\t" \
  "v_mfma_f32_16x16x32_bf16 %0, v[212:215], v[200:203], %0\n\t" \
  "ds_read_b128 v[240:243], %23 offset:0\n\t" \
  "ds_read_b64_tr_b16 v[228:229], %19 offset:10240\n\t" \
  "v_mfma_f32_16x16x32_bf16 %1, v[212:215], v[204:207], %1\n\t" \
  "ds_read_b64_tr_b16 v[230:231], %19 offset:11520\n\t" \
  "ds_read_b64_tr_b16 v[232:233], %20 offset:10240\n\t" \
  "v_mfma_f32_16x16x32_bf16 %2, v[212:215], v[208:211], %2\n\t" \
  "ds_read_b64_tr_b16 v[234:235], %20 offset:11520\n\t" \
  "ds_read_b64_tr_b16 v[236:237], %21 offset:10240\n\t" \
  "v_mfma_f32_16x16x32_bf16 %3, v[216:219], v[200:203], %3\n\t" \
  "ds_read_b64_tr_b16 v[238:239], %21 offset:11520\n\t" \
  "ds_read_b128 v[244:247], %23 offset:2048\n\t" \
  "v_mfma_f32_16x16x32_bf16 %4, v[216:219], v[204:207], %4\n\t" \
  "ds_read_b128 v[248:251], %23 offset:4096\n\t" \
  "ds_read_b128 v[252:255], %23 offset:6144\n\t" \
  "v_mfma_f32_16x16x32_bf16 %5, v[216:219], v[208:211], %5\n\t" \
  "v_add_u32 %19, %24, %19\n\t" \
  "v_mfma_f32_16x16x32_bf16 %6, v[220:223], v[200:203], %6\n\t" \
  "v_add_u32 %20, %24, %20\n\t" \
  "v_mfma_f32_16x16x32_bf16 %7, v[220:223], v[204:207], %7\n\t" \
  "v_add_u32 %21, %24, %21\n\t" \
  "v_mfma_f32_16x16x32_bf16 %8, v[220:223], v[208:211], %8\n\t" \
  "v_add_u32 %22, %24, %22\n\t" \
  "v_mfma_f32_16x16x32_bf16 %9, v[224:227], v[200:203], %9\n\t" \
  "v_add_u32 %23, %24, %23\n\t" \
  "v_mfma_f32_16x16x32_bf16 %10, v[224:227], v[204:207], %10\n\t" \
  "v_mfma_f32_16x16x32_bf16 %11, v[224:227], v[208:211], %11\n\t" \
  "s_waitcnt lgkmcnt(0)\n\t" \
  "s_barrier\n\t" \
  "v_mfma_f32_16x16x32_bf16 %0, v[240:243], v[228:231], %0\n\t" \
  "ds_read_b128 v[212:215], %22 offset:0\n\t" \
  "ds_read_b64_tr_b16 v[200:201], %19 offset:0\n\t" \
  "v_mfma_f32_16x16x32_bf16 %1, v[240:243], v[232:235], %1\n\t" \
  "ds_read_b64_tr_b16 v[202:203], %19 offset:1280\n\t" \
  "ds_read_b64_tr_b16 v[204:205], %20 offset:0\n\t" \
  "v_mfma_f32_16x16x32_bf16 %2, v[240:243], v[236:239], %2\n\t" \
  "ds_read_b64_tr_b16 v[206:207], %20 offset:1280\n\t" \
  "ds_read_b64_tr_b16 v[208:209], %21 offset:0\n\t" \
  "v_mfma_f32_16x16x32_bf16 %3, v[244:247], v[228:231], %3\n\t" \
  "ds_read_b64_tr_b16 v[210:211], %21 offset:1280\n\t" \
  "ds_read_b128 v[216:219], %22 offset:2048\n\t" \
  "v_mfma_f32_16x16x32_bf16 %4, v[244:247], v[232:235], %4\n\t" \
  "ds_read_b128 v[220:223], %22 offset:4096\n\t" \
  "ds_read_b128 v[224:227], %22 offset:6144\n\t" \
  "v_mfma_f32_16x16x32_bf16 %5, v[244:247], v[236:239], %5\n\t" \
  "v_mfma_f32_16x16x32_bf16 %6, v[248:251], v[228:231], %6\n\t" \
  "v_mfma_f32_16x16x32_bf16 %7, v[248:251], v[232:235], %7\n\t" \
  "v_mfma_f32_16x16x32_bf16 %8, v[248:251], v[236:239], %8\n\t" \
  "v_mfma_f32_16x16x32_bf16 %9, v[252:255], v[228:231], %9\n\t" \
  "v_mfma_f32_16x16x32_bf16 %10, v[252:255], v[232:235], %10\n\t" \
  "v_mfma_f32_16x16x32_bf16 %11, v[252:255], v[236:239], %11\n\t" \
  "s_waitcnt lgkmcnt(0)\n\t" \
  "s_nop 7\n\t"

#define ASM_CHUNK_B0 \
  "s_nop 1\n\t" \
  "s_waitcnt lgkmcnt(0)\n\t" \
  "v_mfma_f32_16x16x32_bf16 %0, v[212:215], v[200:203], 0\n\t" \
  "ds_read_b128 v[240:243], %23 offset:0\n\t" \
  "ds_read_b64_tr_b16 v[228:229], %19 offset:10240\n\t" \
  "v_mfma_f32_16x16x32_bf16 %1, v[212:215], v[204:207], 0\n\t" \
  "ds_read_b64_tr_b16 v[230:231], %19 offset:11520\n\t" \
  "ds_read_b64_tr_b16 v[232:233], %20 offset:10240\n\t" \
  "v_mfma_f32_16x16x32_bf16 %2, v[212:215], v[208:211], 0\n\t" \
  "ds_read_b64_tr_b16 v[234:235], %20 offset:11520\n\t" \
  "ds_read_b64_tr_b16 v[236:237], %21 offset:10240\n\t" \
  "v_mfma_f32_16x16x32_bf16 %3, v[216:219], v[200:203], 0\n\t" \
  "ds_read_b64_tr_b16 v[238:239], %21 offset:11520\n\t" \
  "ds_read_b128 v[244:247], %23 offset:2048\n\t" \
  "v_mfma_f32_16x16x32_bf16 %4, v[216:219], v[204:207], 0\n\t" \
  "ds_read_b128 v[248:251], %23 offset:4096\n\t" \
  "ds_read_b128 v[252:255], %23 offset:6144\n\t" \
  "v_mfma_f32_16x16x32_bf16 %5, v[216:219], v[208:211], 0\n\t" \
  "v_add_u32 %19, %24, %19\n\t" \
  "v_mfma_f32_16x16x32_bf16 %6, v[220:223], v[200:203], 0\n\t" \
  "v_add_u32 %20, %24, %20\n\t" \
  "v_mfma_f32_16x16x32_bf16 %7, v[220:223], v[204:207], 0\n\t" \
  "v_add_u32 %21, %24, %21\n\t" \
  "v_mfma_f32_16x16x32_bf16 %8, v[220:223], v[208:211], 0\n\t" \
  "v_add_u32 %22, %24, %22\n\t" \
  "v_mfma_f32_16x16x32_bf16 %9, v[224:227], v[200:203], 0\n\t" \
  "v_add_u32 %23, %24, %23\n\t" \
  "v_mfma_f32_16x16x32_bf16 %10, v[224:227], v[204:207], 0\n\t" \
  "v_mfma_f32_16x16x32_bf16 %11, v[224:227], v[208:211], 0\n\t" \
  "s_waitcnt lgkmcnt(0)\n\t" \
  "s_barrier\n\t" \
  "v_mfma_f32_16x16x32_bf16 %0, v[240:243], v[228:231], %0\n\t" \
  "ds_read_b128 v[212:215], %22 offset:0\n\t" \
  "ds_read_b64_tr_b16 v[200:201], %19 offset:0\n\t" \
  "v_mfma_f32_16x16x32_bf16 %1, v[240:243], v[232:235], %1\n\t" \
  "ds_read_b64_tr_b16 v[202:203], %19 offset:1280\n\t" \
  "ds_read_b64_tr_b16 v[204:205], %20 offset:0\n\t" \
  "v_mfma_f32_16x16x32_bf16 %2, v[240:243], v[236:239], %2\n\t" \
  "ds_read_b64_tr_b16 v[206:207], %20 offset:1280\n\t" \
  "ds_read_b64_tr_b16 v[208:209], %21 offset:0\n\t" \
  "v_mfma_f32_16x16x32_bf16 %3, v[244:247], v[228:231], %3\n\t" \
  "ds_read_b64_tr_b16 v[210:211], %21 offset:1280\n\t" \
  "ds_read_b128 v[216:219], %22 offset:2048\n\t" \
  "v_mfma_f32_16x16x32_bf16 %4, v[244:247], v[232:235], %4\n\t" \
  "ds_read_b128 v[220:223], %22 offset:4096\n\t" \
  "ds_read_b128 v[224:227], %22 offset:6144\n\t" \
  "v_mfma_f32_16x16x32_bf16 %5, v[244:247], v[236:239], %5\n\t" \
  "v_mfma_f32_16x16x32_bf16 %6, v[248:251], v[228:231], %6\n\t" \
  "v_mfma_f32_16x16x32_bf16 %7, v[248:251], v[232:235], %7\n\t" \
  "v_mfma_f32_16x16x32_bf16 %8, v[248:251], v[236:239], %8\n\t" \
  "v_mfma_f32_16x16x32_bf16 %9, v[252:255], v[228:231], %9\n\t" \
  "v_mfma_f32_16x16x32_bf16 %10, v[252:255], v[232:235], %10\n\t" \
  "v_mfma_f32_16x16x32_bf16 %11, v[252:255], v[236:239], %11\n\t" \
  "s_waitcnt lgkmcnt(0)\n\t" \
  "s_nop 7\n\t"

#define ASM_LOAD_A \
  "ds_read_b64_tr_b16 v[200:201], %7 offset:0\n\t" \
  "ds_read_b64_tr_b16 v[202:203], %7 offset:1536\n\t" \
  "ds_read_b128 v[212:215], %10 offset:0\n\t" \
  "ds_read_b128 v[216:219], %10 offset:2048\n\t" \
  "ds_read_b128 v[220:223], %10 offset:4096\n\t" \
  "ds_read_b128 v[224:227], %10 offset:6144\n\t" \
  "ds_read_b64_tr_b16 v[204:205], %8 offset:0\n\t" \
  "ds_read_b64_tr_b16 v[206:207], %8 offset:1536\n\t" \
  "ds_read_b64_tr_b16 v[208:209], %9 offset:0\n\t" \
  "ds_read_b64_tr_b16 v[210:211], %9 offset:1536\n\t"

#define ASM_LOAD_B \
  "ds_read_b128 v[212:215], %10 offset:0\n\t" \
  "ds_read_b64_tr_b16 v[200:201], %7 offset:0\n\t" \
  "ds_read_b64_tr_b16 v[202:203], %7 offset:1280\n\t" \
  "ds_read_b64_tr_b16 v[204:205], %8 offset:0\n\t" \
  "ds_read_b64_tr_b16 v[206:207], %8 offset:1280\n\t" \
  "ds_read_b64_tr_b16 v[208:209], %9 offset:0\n\t" \
  "ds_read_b64_tr_b16 v[210:211], %9 offset:1280\n\t" \
  "ds_read_b128 v[216:219], %10 offset:2048\n\t" \
  "ds_read_b128 v[220:223], %10 offset:4096\n\t" \
  "ds_read_b128 v[224:227], %10 offset:6144\n\t"

struct Geo { unsigned b, g, j, h, c0, uo, d0; };

DI void dma_wave_w(const Geo& G, unsigned dw, const void* kc, const void* wq, const void* wo, unsigned lane, unsigned wave) {
  const __amdgpu_buffer_rsrc_t rA = __builtin_amdgcn_make_buffer_rsrc((void*)wq, 0, DM * NH * DH * 2, 0x00020000);
  unsigned vA[6], rr[3], cc[3];
#pragma unroll
  for (int m = 0; m < 3; ++m) {
    const unsigned u = 64 * m + lane, r = (u * 2731) >> 16, c = u - 24 * r;
    rr[m] = r; cc[m] = c;
    vA[m] = r * 12288 + ((c ^ (r & 2)) << 4);
    vA[m + 3] = (vA[m] ^ 64) + 8 * 12288;
  }
  unsigned kcx = G.c0;
  auto issueA = [&](unsigned n) {
    const unsigned slot = (n & 3) * SLOT + dw * 12288, so = (kcx * 64 + dw * 32) * 12288 + G.h * 384;
#pragma unroll
    for (int p = 0; p < 12; ++p) dma16(rA, slot + p * 1024, vA[p % 6], so + (p / 6) * 16 * 12288);
    kcx = kcx == 9 ? 0u : kcx + 1;
  };
  issueA(0); issueA(1);
  WAITVM(12); BAR();
  STAMP(1);
  {
    const __amdgpu_buffer_rsrc_t rK = __builtin_amdgcn_make_buffer_rsrc((void*)kc, 0, NB * NH * SK * DH * 2, 0x00020000);
    const unsigned so = ((G.b * 32 + G.h) * 64 + dw * 32) * 384;
#pragma unroll
    for (int p = 0; p < 12; ++p) {
      const unsigned m = p % 3, vk = rr[m] * 384 + ((cc[m] ^ (rr[m] >> 1)) << 4);
      dma16(rK, PBUF + dw * 12288 + p * 1024, (p % 6) < 3 ? vk : (vk ^ 64) + 8 * 384, so + (p / 6) * 16 * 384);
    }
  }
  issueA(2);
  WAITVM(24); BAR();
  issueA(3);
  const __amdgpu_buffer_rsrc_t rB = __builtin_amdgcn_make_buffer_rsrc((void*)wo, 0, DM * NH * DH * 2, 0x00020000);
  unsigned vB[5];
#pragma unroll
  for (int m = 0; m < 5; ++m) {
    const unsigned u = 64 * m + lane, r = (u * 3277) >> 16, c = u - 20 * r;
    vB[m] = r * 1280 + ((c ^ ((r >> 2) & 2)) << 4);
  }
  unsigned hh = (G.j - G.uo) & 3, dc = G.d0, o3 = 0;
  const unsigned sb = (768 * G.g + dw * 32) * 1280 + 320 * G.j;
  auto issueB = [&](unsigned n) {
    const unsigned slot = (n & 3) * SLOT + dw * 10240, so = sb + (192 * hh + 64 * dc) * 1280;
#pragma unroll
    for (int p = 0; p < 10; ++p) dma16(rB, slot + p * 1024, vB[p % 5], so + (p / 5) * 16 * 1280);
    dc = dc == 2 ? 0u : dc + 1;
    hh = o3 == 2 ? (hh + 1) & 3 : hh;
    o3 = o3 == 2 ? 0u : o3 + 1;
  };
#pragma nounroll
  for (unsigned t = 1; t < 7; ++t) { issueA(t + 3); STAMPD(16 + 4 * t); WAITVM(24); STAMPD(17 + 4 * t); BAR(); }
  issueB(10); STAMPD(16 + 28); WAITVM(22); STAMPD(17 + 28); BAR();
  issueB(11); STAMPD(16 + 32); WAITVM(20); STAMPD(17 + 32); BAR();
  issueB(12); BAR();
  BAR();
  WAITVM(20); BAR();
#pragma nounroll
  for (unsigned t = 11; t < 20; ++t) { issueB(t + 2); STAMPD(16 + 4 * t); WAITVM(20); STAMPD(17 + 4 * t); BAR(); }
  WAITVM(10); BAR();
  WAITVM(0); BAR();
  BAR();
  STAMP(3);
}
DI void dma_wave_a(const Geo& G, const void* x, const void* vc, unsigned lane, unsigned wave) {
  const __amdgpu_buffer_rsrc_t rA = __builtin_amdgcn_make_buffer_rsrc((void*)x, 0, NB * SQ * DM * 2, 0x00020000);
  const unsigned r = lane >> 3, c = lane & 7, cs = (c ^ (r >> 1)) << 4;
  const unsigned vA0 = r * 1280 + cs, vA1 = r * 1280 + (cs ^ 64);
  unsigned kcx = G.c0;
  auto issueA = [&](unsigned n) {
    const unsigned slot = (n & 3) * SLOT + AUX, so = G.b * 64 * 1280 + kcx * 128;
#pragma unroll
    for (int p = 0; p < 8; ++p) dma16(rA, slot + p * 1024, (p & 1) ? vA1 : vA0, so + p * 8 * 1280);
    kcx = kcx == 9 ? 0u : kcx + 1;
  };
  issueA(0); issueA(1);
  WAITVM(8); BAR();
  issueA(2);
  WAITVM(8); BAR();
  issueA(3);
  const __amdgpu_buffer_rsrc_t rB = __builtin_amdgcn_make_buffer_rsrc((void*)vc, 0, NB * NH * SK * DH * 2, 0x00020000);
  const unsigned vB0 = r * 384 + cs, vB1 = r * 384 + (cs ^ 64);
  unsigned hh = (G.j - G.uo) & 3, dc = G.d0, o3 = 0;
  auto issueB = [&](unsigned n) {
    const unsigned slot = (n & 3) * SLOT + AUX, so = (G.b * 32 + 4 * G.g + hh) * 24576 + dc * 128;
#pragma unroll
    for (int p = 0; p < 8; ++p) dma16(rB, slot + p * 1024, (p & 1) ? vB1 : vB0, so + p * 8 * 384);
    dc = dc == 2 ? 0u : dc + 1;
    hh = o3 == 2 ? (hh + 1) & 3 : hh;
    o3 = o3 == 2 ? 0u : o3 + 1;
  };
#pragma nounroll
  for (unsigned t = 1; t < 9; ++t) { if (t < 7) issueA(t + 3); else issueB(t + 3); WAITVM(16); BAR(); }
  issueB(12); BAR();
  BAR();
  WAITVM(16); BAR();
#pragma nounroll
  for (unsigned t = 11; t < 20; ++t) { issueB(t + 2); WAITVM(16); BAR(); }
  WAITVM(8); BAR();
  WAITVM(0); BAR();
  BAR();
  STAMP(3);
}

DI void reduce_tile(const Geo& G, unsigned dt, u64 tagR, char* ws, void* out, unsigned lane, bool upd, unsigned wave) {
  const unsigned nt = dt >> 1, ip = dt & 1;
  const unsigned pw = nt < 3 ? 0u : nt < 6 ? 1u : nt < 8 ? 2u : 3u;
  const char* fl = ws + WS_FR + (size_t)((((G.b * 4 + G.j) * 8 + (lane & 7)) * 4 + pw) * 128);
#pragma nounroll
  for (unsigned sp = 0; sp < SPIN_CAP; ++sp) {
    u64 v = ld_sc1_64(fl);
    if (__all(v == tagR)) break;
  }
  CBAR();
  STAMP(4);
  __amdgpu_buffer_rsrc_t rR = __builtin_amdgcn_make_buffer_rsrc((void*)(ws + WS_R), 0, 256 * 20480, 0x00020000);
  const unsigned base = ((G.b * 4 + G.j) * 8) * 20480 + dt * 1024;
  u32x4 v[8];
#pragma unroll
  for (int s = 0; s < 8; ++s) v[s] = __builtin_amdgcn_raw_buffer_load_b128(rR, lane * 16, base + s * 20480, LD_AUX);
  float a[8];
#pragma unroll
  for (int k = 0; k < 4; ++k) { a[2 * k] = __uint_as_float(v[0][k] << 16); a[2 * k + 1] = __uint_as_float(v[0][k] & 0xffff0000u); }
#pragma unroll
  for (int s = 1; s < 8; ++s)
#pragma unroll
    for (int k = 0; k < 4; ++k) { a[2 * k] += __uint_as_float(v[s][k] << 16); a[2 * k + 1] += __uint_as_float(v[s][k] & 0xffff0000u); }
  __amdgpu_buffer_rsrc_t rO = __builtin_amdgcn_make_buffer_rsrc(out, 0, NB * SQ * DM * 2, 0x00020000);
  const unsigned row = G.b * 64 + 32 * ip + (lane & 15), col = 160 * G.j + 16 * nt + 4 * (lane >> 4);
  u32x2 o0 = {cvtpk(a[0], a[1]), cvtpk(a[2], a[3])}, o1 = {cvtpk(a[4], a[5]), cvtpk(a[6], a[7])};
  __builtin_amdgcn_raw_buffer_store_b64(o0, rO, (row * DM + col) * 2, 0, 0);
  __builtin_amdgcn_raw_buffer_store_b64(o1, rO, ((row + 16) * DM + col) * 2, 0, 0);
  if (upd && lane == 0) st_sc1_64(ws + WS_ER + (G.b * 4 + G.j) * 128, tagR);
}

struct Frag { u32x4 f[7]; };
#define F0_OPS(F) "+{v[200:203]}"(F.f[0]), "+{v[204:207]}"(F.f[1]), "+{v[208:211]}"(F.f[2]), "+{v[212:215]}"(F.f[3]), "+{v[216:219]}"(F.f[4]), "+{v[220:223]}"(F.f[5]), "+{v[224:227]}"(F.f[6])
#define F1_CLOB "v228", "v229", "v230", "v231", "v232", "v233", "v234", "v235", "v236", "v237", "v238", "v239", "v240", "v241", "v242", "v243", "v244", "v245", "v246", "v247", "v248", "v249", "v250", "v251", "v252", "v253", "v254", "v255"
#define RUN_CHUNK(TXT, acc, F, ad, delta) asm volatile(TXT \
    : "+v"(acc[0]), "+v"(acc[1]), "+v"(acc[2]), "+v"(acc[3]), "+v"(acc[4]), "+v"(acc[5]), "+v"(acc[6]), "+v"(acc[7]), "+v"(acc[8]), "+v"(acc[9]), "+v"(acc[10]), "+v"(acc[11]), \
      F0_OPS(F), "+v"(ad[0]), "+v"(ad[1]), "+v"(ad[2]), "+v"(ad[3]), "+v"(ad[4]) : "s"(delta) : F1_CLOB, "memory")
#define RUN_CHUNK0(TXT, acc, F, ad, delta) asm volatile(TXT \
    : "=&v"(acc[0]), "=&v"(acc[1]), "=&v"(acc[2]), "=&v"(acc[3]), "=&v"(acc[4]), "=&v"(acc[5]), "=&v"(acc[6]), "=&v"(acc[7]), "=&v"(acc[8]), "=&v"(acc[9]), "=&v"(acc[10]), "=&v"(acc[11]), \
      F0_OPS(F), "+v"(ad[0]), "+v"(ad[1]), "+v"(ad[2]), "+v"(ad[3]), "+v"(ad[4]) : "s"(delta) : F1_CLOB, "memory")
#define RUN_LOAD(TXT, F, ad) asm volatile(TXT : F0_OPS(F) : "v"(ad[0]), "v"(ad[1]), "v"(ad[2]), "v"(ad[3]) : "memory")
DI unsigned slot_delta(unsigned c) { return (c & 3) == 3 ? (unsigned)(-3 * (int)SLOT) : SLOT; }

__global__ void __launch_bounds__(512, 2)
mha_fused(const void* __restrict__ x, const void* __restrict__ kc, const void* __restrict__ vc, const void* __restrict__ wq,
          const void* __restrict__ wo, void* __restrict__ out, char* __restrict__ ws) {
  const unsigned tid = threadIdx.x, lane = tid & 63, wave = rfl(tid >> 6);
  Geo G;
  { const unsigned bid = blockIdx.x; G.g = bid & 7; const unsigned idx = bid >> 3; G.j = idx & 3; G.b = idx >> 2; G.h = 4 * G.g + G.j;
    const unsigned fb = (G.b * 10) >> 3, oj = (G.j * 10 + 2) >> 2;
    unsigned c0 = fb + oj; if (c0 >= 10) c0 -= 10;
    G.c0 = ROTATE ? c0 : 0u;
    G.uo = ROTB ? (G.b * 11) >> 5 : 0u; G.d0 = ROTB ? G.b - 3 * G.uo : 0u; }
  STAMP_BEGIN();
  if (wave >= 4 && wave < 7) {
    if (wave < 6) dma_wave_w(G, wave - 4, kc, wq, wo, lane, wave);
    else dma_wave_a(G, x, vc, lane, wave);
    const u64 tagR = lds_u64(MISC + L_TAGR);
    const unsigned dt = G.g + 8 * (wave - 4);
    if (dt < 20) reduce_tile(G, dt, tagR, ws, out, lane, wave == 4 && G.g == 0, wave);
    STAMP(5);
  } else if (wave == 7) {
    u64 eP, eR;
    asm volatile("global_load_dwordx2 %0, %1, off sc1" : "=v"(eP) : "v"(ws + WS_EP + (G.b * 8 + G.g) * 128) : "memory");
    asm volatile("global_load_dwordx2 %0, %1, off sc1" : "=v"(eR) : "v"(ws + WS_ER + (G.b * 4 + G.j) * 128) : "memory");
    BAR(); BAR(); BAR(); BAR(); BAR(); BAR(); BAR();
    asm volatile("s_waitcnt vmcnt(0)" : "+v"(eP), "+v"(eR) :: "memory");
    const u64 tagP = next_tag(eP), tagR = next_tag(eR);
    if (lane == 0) { lds_st32(MISC + L_TAGP, (unsigned)tagP); lds_st32(MISC + L_TAGP + 4, (unsigned)(tagP >> 32));
                     lds_st32(MISC + L_TAGR, (unsigned)tagR); lds_st32(MISC + L_TAGR + 4, (unsigned)(tagR >> 32)); }
    BAR_L(); BAR(); BAR(); BAR();
    BAR(); BAR(); BAR(); BAR();
    STAMP(1);
    const __amdgpu_buffer_rsrc_t rP = __builtin_amdgcn_make_buffer_rsrc((void*)(ws + WS_P), 0, 256 * 8192, 0x00020000);
    const char* fl = ws + WS_FP + (size_t)((G.b * 32 + 4 * G.g) * 4 + (lane & 15)) * 128;
    unsigned s = 13;
    {
      u64 pv;
      asm volatile("global_load_dwordx2 %0, %1, off sc1" : "=v"(pv) : "v"(fl) : "memory");
#pragma nounroll
      for (unsigned sp = 0; sp < SPIN_CAP; ++sp) {
        if (s == 20 || vm_idle()) {
          asm volatile("s_waitcnt vmcnt(0)" : "+v"(pv) :: "memory");
          if (__all(pv == tagP)) break;
          asm volatile("global_load_dwordx2 %0, %1, off sc1" : "=v"(pv) : "v"(fl) : "memory");
        }
        if (s < 20) { BAR(); ++s; }
      }
      asm volatile("s_waitcnt vmcnt(0)" : "+v"(pv) :: "memory");
    }
    STAMP(2);
    auto fetchP = [&](unsigned u, unsigned buf) {
      const unsigned hh = (G.j - G.uo + u) & 3;
#pragma unroll
      for (int i = 0; i < 8; ++i) {
        const unsigned dst = buf == 3 ? (i >> 2) * SLOT + 20480 + (i & 3) * 1024 : PBUF + (i >> 1) * 6144 + buf * 2048 + (i & 1) * 1024;
        __builtin_amdgcn_raw_ptr_buffer_load_lds(rP, (LAS void*)(uintptr_t)dst, 16, lane * 16, (G.b * 32 + 4 * G.g + hh) * 8192 + i * 1024, 0, LD_AUX);
      }
    };
    fetchP(G.uo == 0 ? 1u : 0u, 1);
    fetchP(G.uo == 2 ? 1u : 2u, 2);
    fetchP(3, 3);
    if (G.j == 0 && lane == 0) st_sc1_64(ws + WS_EP + (G.b * 8 + G.g) * 128, tagP);
    STAMP(3);
#pragma nounroll
    while (s < 20) { BAR(); ++s; }
    WAITVM(0);
    STAMP(4);
    BAR(); ++s;
    BAR(); BAR();
    STAMP(6);
  } else {
    const unsigned i16 = lane & 15, g4 = lane >> 4, q = i16 >> 2, p = lane & 3;
    const unsigned a0 = AUX + i16 * 128 + ((g4 ^ (i16 >> 1)) << 4);
    Frag F0;
#pragma unroll
    for (int i = 0; i < 7; ++i) F0.f[i] = (u32x4){0, 0, 0, 0};
    unsigned ad[5];
    f32x4 acc[12];
#pragma unroll
    for (int t = 0; t < 12; ++t) acc[t] = (f32x4){0.f, 0.f, 0.f, 0.f};
    { const unsigned phi = ((q >> 1) & 1) | ((g4 & 1) << 1);
#pragma unroll
      for (int e = 0; e < 3; ++e) ad[e] = (8 * g4 + q) * 384 + 32 * ((3 * wave + e) ^ phi) + 16 * (p >> 1) + 8 * (p & 1);
      ad[3] = a0; ad[4] = a0 ^ 64; }
    asm volatile("" : "+v"(acc[0]), "+v"(acc[1]), "+v"(acc[2]), "+v"(acc[3]), "+v"(acc[4]), "+v"(acc[5]), "+v"(acc[6]), "+v"(acc[7]), "+v"(acc[8]), "+v"(acc[9]), "+v"(acc[10]), "+v"(acc[11]),
                      "+v"(ad[0]), "+v"(ad[1]), "+v"(ad[2]), "+v"(ad[3]), "+v"(ad[4]));
    BAR();
    STAMP(1);
    RUN_LOAD(ASM_LOAD_A, F0, ad);
#pragma nounroll
    for (unsigned c = 0; c < 10; ++c) { const unsigned dl = slot_delta(c); RUN_CHUNK(ASM_CHUNK_A, acc, F0, ad, dl); }
    STAMP(2);
    {
      const unsigned qw = opaque(SLOT + ((g4 >> 1) * 16 + i16) * 16 + (g4 & 1) * 8);
#pragma unroll
      for (int e = 0; e < 3; ++e) {
        const unsigned tau = 3 * wave + e;
        const unsigned qe = qw + (tau >> 1) * 1024 + (tau & 1) * 512;
#pragma unroll
        for (int t = 0; t < 4; ++t) { u32x2 v = {cvtpk(acc[4 * e + t][0], acc[4 * e + t][1]), cvtpk(acc[4 * e + t][2], acc[4 * e + t][3])};
          *(LAS u32x2*)(uintptr_t)(qe + t * 6144) = v; }
      }
    }
    BAR_L();
    STAMP(3);
    f32x4 st[4];
    {
      const unsigned qr = opaque(SLOT + wave * 6144 + lane * 16);
      bf16x8 qf[6];
#pragma unroll
      for (int s = 0; s < 6; ++s) qf[s] = lds_b128(qr + s * 1024);
      const unsigned slot = PBUF, fk = i16 >> 1;
      unsigned ka[2];
#pragma unroll
      for (int so = 0; so < 2; ++so) ka[so] = opaque(slot + i16 * 384 + (((4 * so + g4) ^ fk) << 4));
      bf16x8 kf[6][4];
#pragma unroll
      for (int s = 0; s < 6; ++s)
#pragma unroll
        for (int kt = 0; kt < 4; ++kt) kf[s][kt] = lds_b128(ka[s & 1] + kt * 16 * 384 + 128 * (s >> 1));
      BAR_L();
      SB0();
#pragma unroll
      for (int kt = 0; kt < 4; ++kt) st[kt] = (f32x4){0.f, 0.f, 0.f, 0.f};
#pragma unroll
      for (int s = 0; s < 6; ++s)
#pragma unroll
        for (int kt = 0; kt < 4; ++kt) st[kt] = MFMA(kf[s][kt], qf[s], st[kt]);
    }
    SB0();
    const unsigned ntb = wave == 3 ? 7u : 3u * wave;
    { const unsigned slot = (10 & 3) * SLOT;
#pragma unroll
      for (int e = 0; e < 3; ++e) ad[e] = slot + (8 * g4 + q) * 320 + 32 * ((ntb + e) ^ (g4 & 1)) + 16 * (p >> 1) + 8 * (p & 1);
      ad[3] = slot + a0; ad[4] = slot + (a0 ^ 64); }
    RUN_LOAD(ASM_LOAD_B, F0, ad);
    asm volatile("s_nop 7" : "+v"(st[0]), "+v"(st[1]), "+v"(st[2]), "+v"(st[3]));
    const __amdgpu_buffer_rsrc_t rP = __builtin_amdgcn_make_buffer_rsrc((void*)(ws + WS_P), 0, 256 * 8192, 0x00020000);
    {
      float m = __builtin_amdgcn_fmed3f(st[0][0], st[0][1], __builtin_inff());
      m = max3f(m, st[0][2], st[0][3]);
#pragma unroll
      for (int kt = 1; kt < 4; ++kt) { m = max3f(m, st[kt][0], st[kt][1]); m = max3f(m, st[kt][2], st[kt][3]); }
      { auto r1 = __builtin_amdgcn_permlane16_swap(__float_as_uint(m), __float_as_uint(m), false, false);
        m = max3f(__uint_as_float(r1[0]), __uint_as_float(r1[1]), __uint_as_float(r1[1]));
        auto r2 = __builtin_amdgcn_permlane32_swap(__float_as_uint(m), __float_as_uint(m), false, false);
        m = max3f(__uint_as_float(r2[0]), __uint_as_float(r2[1]), __uint_as_float(r2[1])); }
      constexpr float SC = 0.07216878364870322f * 1.4426950408889634f;
      const float ml = m * SC;
      float sum = 0.f;
#pragma unroll
      for (int kt = 0; kt < 4; ++kt)
#pragma unroll
        for (int r = 0; r < 4; ++r) { const float e = __builtin_amdgcn_exp2f(__builtin_fmaf(st[kt][r], SC, -ml)); st[kt][r] = e; sum += e; }
      { auto r1 = __builtin_amdgcn_permlane16_swap(__float_as_uint(sum), __float_as_uint(sum), false, false);
        sum = __uint_as_float(r1[0]) + __uint_as_float(r1[1]);
        auto r2 = __builtin_amdgcn_permlane32_swap(__float_as_uint(sum), __float_as_uint(sum), false, false);
        sum = __uint_as_float(r2[0]) + __uint_as_float(r2[1]); }
      const float inv = __builtin_amdgcn_rcpf(sum);
#pragma unroll
      for (int kt = 0; kt < 4; ++kt) st[kt] *= inv;
      const unsigned pw = opaque(PBUF + wave * 6144 + lane * 16);
#pragma unroll
      for (int s2 = 0; s2 < 2; ++s2) {
        const u32x4 pf = __builtin_bit_cast(u32x4, pack8(st[2 * s2], st[2 * s2 + 1]));
        __builtin_amdgcn_raw_buffer_store_b128(pf, rP, lane * 16, (G.b * 32 + G.h) * 8192 + (2 * wave + s2) * 1024, ST_AUX);
        *(LAS u32x4*)(uintptr_t)(pw + s2 * 1024) = pf;
      }
    }
    STAMP(4);
    f32x4 oa[4][3];
#pragma unroll
    for (int it = 0; it < 4; ++it)
#pragma unroll
      for (int e = 0; e < 3; ++e) oa[it][e] = (f32x4){0.f, 0.f, 0.f, 0.f};
    auto pvw = [&](unsigned buf, const bf16x8 (&pk)[2][3]) {
      const unsigned pb = opaque((buf == 3 ? 20480u : PBUF + buf * 2048) + lane * 16);
      bf16x8 pf[4][2];
#pragma unroll
      for (int s2 = 0; s2 < 2; ++s2)
#pragma unroll
        for (int it = 0; it < 4; ++it) pf[it][s2] = lds_b128(pb + (buf == 3 ? (it >> 1) * SLOT + (it & 1) * 2048 : it * 6144) + s2 * 1024);
      SB0();
#pragma unroll
      for (int s2 = 0; s2 < 2; ++s2)
#pragma unroll
        for (int it = 0; it < 4; ++it)
#pragma unroll
          for (int e = 0; e < 3; ++e) oa[it][e] = MFMA(pk[s2][e], pf[it][s2], oa[it][e]);
      SB0();
    };
    f32x4 vw[12];
    auto pack_vw = [&](bf16x8 (&pk)[2][3]) {
#pragma unroll
      for (int s2 = 0; s2 < 2; ++s2)
#pragma unroll
        for (int e = 0; e < 3; ++e) pk[s2][e] = pack8(vw[3 * (2 * s2) + e], vw[3 * (2 * s2 + 1) + e]);
    };
    auto head_first = [&](unsigned c) { const unsigned dl = slot_delta(c); RUN_CHUNK0(ASM_CHUNK_B0, vw, F0, ad, dl); };
    auto head_next = [&](unsigned c) { const unsigned dl = slot_delta(c); RUN_CHUNK(ASM_CHUNK_B, vw, F0, ad, dl); };
    bf16x8 pk1[2][3], pk2[2][3];
    head_first(10);
    head_next(11);
    {
      const u64 tagP = lds_u64(MISC + L_TAGP);
      WAITVM(0);
      if (lane == 0) st_sc1_64(ws + WS_FP + (size_t)((G.b * 32 + G.h) * 4 + wave) * 128, tagP); }
    head_next(12);
    STAMP(5);
    pack_vw(pk1);
    if (G.uo == 0) pvw(0, pk1);
    head_first(13); head_next(14); head_next(15);
    STAMP(6);
    pack_vw(pk2);
    if (G.uo == 1) pvw(0, pk2);
    if (G.uo == 0) {
#pragma unroll
      for (int s2 = 0; s2 < 2; ++s2)
#pragma unroll
        for (int e = 0; e < 3; ++e) pk1[s2][e] = pk2[s2][e];
    }
    head_first(16); head_next(17); head_next(18);
    STAMP(7);
    if (G.uo == 2) { bf16x8 pk[2][3]; pack_vw(pk); pvw(0, pk); }
    else pack_vw(pk2);
    head_first(19);
    pvw(1, pk1);
    head_next(20);
    pvw(2, pk2);
    head_next(21);
    STAMP(8);
    pack_vw(pk1); pvw(3, pk1);
    STAMP(9);
    const u64 tagR = lds_u64(MISC + L_TAGR);
    __amdgpu_buffer_rsrc_t rR = __builtin_amdgcn_make_buffer_rsrc((void*)(ws + WS_R), 0, 256 * 20480, 0x00020000);
    const unsigned base = ((G.b * 4 + G.j) * 8 + G.g) * 20480;
#pragma unroll
    for (int e = 0; e < 3; ++e) {
      const bool keep = wave < 2 || (wave == 2 ? e < 2 : e > 0);
      if (keep) {
#pragma unroll
        for (int ip = 0; ip < 2; ++ip)
          __builtin_amdgcn_raw_buffer_store_b128(__builtin_bit_cast(u32x4, pack8(oa[2 * ip][e], oa[2 * ip + 1][e])), rR, lane * 16, base + (2 * (ntb + e) + ip) * 1024, ST_AUX);
      }
    }
    WAITVM(0);
    if (lane == 0) st_sc1_64(ws + WS_FR + (size_t)((((G.b * 4 + G.j) * 8 + G.g) * 4 + wave) * 128), tagR);
    STAMP(10);
  }
  STAMP_END();
}

extern "C" void kernel_launch(void* const* d_in, const int* in_sizes, int n_in, void* d_out, int out_size, void* d_ws, size_t ws_size,
                              hipStream_t stream) {
  (void)in_sizes; (void)n_in; (void)out_size; (void)ws_size;
  (void)hipFuncSetAttribute((const void*)mha_fused, hipFuncAttributeMaxDynamicSharedMemorySize, LDS_BYTES);
  mha_fused<<<256, 512, LDS_BYTES, stream>>>(d_in[0], d_in[1], d_in[2], d_in[3], d_in[4], d_out, (char*)d_ws);
}
```

```cpp
#include <hip/hip_runtime.h>
#include <hip/hip_bf16.h>
#include <stdint.h>

#define LAS __attribute__((address_space(3)))
#define GAS __attribute__((address_space(1)))
#define DI __device__ __forceinline__
typedef short s16x4 __attribute__((ext_vector_type(4)));
typedef __bf16 bf16x2 __attribute__((ext_vector_type(2)));
typedef __bf16 bf16x8 __attribute__((ext_vector_type(8)));
typedef float f32x2 __attribute__((ext_vector_type(2)));
typedef float f32x4 __attribute__((ext_vector_type(4)));
typedef unsigned u32x2 __attribute__((ext_vector_type(2)));
typedef unsigned u32x4 __attribute__((ext_vector_type(4)));
typedef unsigned long long u64;

constexpr int NB = 8, SQ = 64, DM = 640, NH = 32, SK = 64, DH = 192;
constexpr unsigned SLOT = 32768, AUX = 24576;
constexpr unsigned PBUF = 131072, MISC = PBUF + 3 * 8192, LDS_USED = MISC + 256;
constexpr unsigned L_TAGP = 0, L_TAGR = 8;
constexpr unsigned WS_EP = 0;
constexpr unsigned WS_ER = 8192;
constexpr unsigned WS_FP = 16384;
constexpr unsigned WS_FR = WS_FP + 131072;
constexpr unsigned WS_P = 524288;
constexpr unsigned WS_R = WS_P + 256 * 8192;
constexpr unsigned SPIN_CAP = 1u << 16;

constexpr unsigned LDS_BYTES = LDS_USED;
constexpr int SC1 = 16;

DI unsigned rfl(unsigned x) { return __builtin_amdgcn_readfirstlane(x); }
DI unsigned cvtpk(float lo, float hi) { f32x2 v = {lo, hi}; bf16x2 b = __builtin_convertvector(v, bf16x2); return __builtin_bit_cast(unsigned, b); }
DI bf16x8 pack8(const f32x4& a, const f32x4& b) {
  u32x4 p = {cvtpk(a[0], a[1]), cvtpk(a[2], a[3]), cvtpk(b[0], b[1]), cvtpk(b[2], b[3])};
  return __builtin_bit_cast(bf16x8, p);
}
#define MFMA(a, b, c) __builtin_amdgcn_mfma_f32_16x16x32_bf16((a), (b), (c), 0, 0, 0)
DI bf16x8 lds_b128(unsigned addr) { return *(const LAS bf16x8*)(uintptr_t)addr; }
DI unsigned lds_u32(unsigned addr) { return *(const volatile LAS unsigned*)(uintptr_t)addr; }
DI void lds_st32(unsigned addr, unsigned v) { *(volatile LAS unsigned*)(uintptr_t)addr = v; }
DI u64 lds_u64(unsigned addr) { return (u64)lds_u32(addr) | ((u64)lds_u32(addr + 4) << 32); }
#define CBAR() asm volatile("" ::: "memory")
#define SB0() __builtin_amdgcn_sched_barrier(0)
#define BAR() asm volatile("s_barrier" ::: "memory")
#define BAR_L() asm volatile("s_waitcnt lgkmcnt(0)\n\ts_barrier" ::: "memory")
#define WAITVM(n) asm volatile("s_waitcnt vmcnt(" #n ")" ::: "memory")
DI void dma16(__amdgpu_buffer_rsrc_t r, unsigned lds, unsigned voff, unsigned soff) {
  __builtin_amdgcn_raw_ptr_buffer_load_lds(r, (LAS void*)(uintptr_t)lds, 16, voff, soff, 0, 0);
}
DI unsigned opaque(unsigned x) { asm volatile("" : "+v"(x)); return x; }
DI float max3f(float a, float b, float c) { float r; asm("v_max3_f32 %0, %1, %2, %3" : "=v"(r) : "v"(a), "v"(b), "v"(c)); return r; }
DI u64 next_tag(u64 e) { return e * 6364136223846793005ull + 1442695040888963407ull; }
DI u64 ld_sc1_64(const void* p) { return __hip_atomic_load((const GAS u64*)p, __ATOMIC_RELAXED, __HIP_MEMORY_SCOPE_AGENT); }
DI void st_sc1_64(void* p, u64 v) { __hip_atomic_store((GAS u64*)p, v, __ATOMIC_RELAXED, __HIP_MEMORY_SCOPE_AGENT); }
DI bool vm_idle() { unsigned r; asm volatile("s_getreg_b32 %0, hwreg(HW_REG_IB_STS)" : "=s"(r) :: "memory"); return (r & 0x00C0000Fu) == 0; }

#define ASM_CHUNK_A \
  "s_nop 1\n\t" \
  "s_waitcnt lgkmcnt(0)\n\t" \
  "v_mfma_f32_16x16x32_bf16 %0, v[200:203], v[212:215], %0\n\t" \
  "ds_read_b64_tr_b16 v[228:229], %19 offset:12288\n\t" \
  "ds_read_b64_tr_b16 v[230:231], %19 offset:13824\n\t" \
  "v_mfma_f32_16x16x32_bf16 %1, v[200:203], v[216:219], %1\n\t" \
  "ds_read_b128 v[240:243], %23 offset:0\n\t" \
  "ds_read_b128 v[244:247], %23 offset:2048\n\t" \
  "v_mfma_f32_16x16x32_bf16 %2, v[200:203], v[220:223], %2\n\t" \
  "ds_read_b128 v[248:251], %23 offset:4096\n\t" \
  "ds_read_b128 v[252:255], %23 offset:6144\n\t" \
  "v_mfma_f32_16x16x32_bf16 %3, v[200:203], v[224:227], %3\n\t" \
  "ds_read_b64_tr_b16 v[232:233], %20 offset:12288\n\t" \
  "ds_read_b64_tr_b16 v[234:235], %20 offset:13824\n\t" \
  "v_mfma_f32_16x16x32_bf16 %4, v[204:207], v[212:215], %4\n\t" \
  "ds_read_b64_tr_b16 v[236:237], %21 offset:12288\n\t" \
  "ds_read_b64_tr_b16 v[238:239], %21 offset:13824\n\t" \
  "v_mfma_f32_16x16x32_bf16 %5, v[204:207], v[216:219], %5\n\t" \
  "v_add_u32 %19, %24, %19\n\t" \
  "v_mfma_f32_16x16x32_bf16 %6, v[204:207], v[220:223], %6\n\t" \
  "v_add_u32 %20, %24, %20\n\t" \
  "v_mfma_f32_16x16x32_bf16 %7, v[204:207], v[224:227], %7\n\t" \
  "v_add_u32 %21, %24, %21\n\t" \
  "v_mfma_f32_16x16x32_bf16 %8, v[208:211], v[212:215], %8\n\t" \
  "v_add_u32 %22, %24, %22\n\t" \
  "v_mfma_f32_16x16x32_bf16 %9, v[208:211], v[216:219], %9\n\t" \
  "v_add_u32 %23, %24, %23\n\t" \
  "v_mfma_f32_16x16x32_bf16 %10, v[208:211], v[220:223], %10\n\t" \
  "v_mfma_f32_16x16x32_bf16 %11, v[208:211], v[224:227], %11\n\t" \
  "s_waitcnt lgkmcnt(0)\n\t" \
  "s_barrier\n\t" \
  "v_mfma_f32_16x16x32_bf16 %0, v[228:231], v[240:243], %0\n\t" \
  "ds_read_b64_tr_b16 v[200:201], %19 offset:0\n\t" \
  "ds_read_b64_tr_b16 v[202:203], %19 offset:1536\n\t" \
  "v_mfma_f32_16x16x32_bf16 %1, v[228:231], v[244:247], %1\n\t" \
  "ds_read_b128 v[212:215], %22 offset:0\n\t" \
  "ds_read_b128 v[216:219], %22 offset:2048\n\t" \
  "v_mfma_f32_16x16x32_bf16 %2, v[228:231], v[248:251], %2\n\t" \
  "ds_read_b128 v[220:223], %22 offset:4096\n\t" \
  "ds_read_b128 v[224:227], %22 offset:6144\n\t" \
  "v_mfma_f32_16x16x32_bf16 %3, v[228:231], v[252:255], %3\n\t" \
  "ds_read_b64_tr_b16 v[204:205], %20 offset:0\n\t" \
  "ds_read_b64_tr_b16 v[206:207], %20 offset:1536\n\t" \
  "v_mfma_f32_16x16x32_bf16 %4, v[232:235], v[240:243], %4\n\t" \
  "ds_read_b64_tr_b16 v[208:209], %21 offset:0\n\t" \
  "ds_read_b64_tr_b16 v[210:211], %21 offset:1536\n\t" \
  "v_mfma_f32_16x16x32_bf16 %5, v[232:235], v[244:247], %5\n\t" \
  "v_mfma_f32_16x16x32_bf16 %6, v[232:235], v[248:251], %6\n\t" \
  "v_mfma_f32_16x16x32_bf16 %7, v[232:235], v[252:255], %7\n\t" \
  "v_mfma_f32_16x16x32_bf16 %8, v[236:239], v[240:243], %8\n\t" \
  "v_mfma_f32_16x16x32_bf16 %9, v[236:239], v[244:247], %9\n\t" \
  "v_mfma_f32_16x16x32_bf16 %10, v[236:239], v[248:251], %10\n\t" \
  "v_mfma_f32_16x16x32_bf16 %11, v[236:239], v[252:255], %11\n\t" \
  "s_waitcnt lgkmcnt(0)\n\t" \
  "s_nop 7\n\t"

#define ASM_CHUNK_B \
  "s_nop 1\n\t" \
  "s_waitcnt lgkmcnt(0)\n\t" \
  "v_mfma_f32_16x16x32_bf16 %0, v[212:215], v[200:203], %0\n\t" \
  "ds_read_b128 v[240:243], %23 offset:0\n\t" \
  "ds_read_b64_tr_b16 v[228:229], %19 offset:10240\n\t" \
  "v_mfma_f32_16x16x32_bf16 %1, v[212:215], v[204:207], %1\n\t" \
  "ds_read_b64_tr_b16 v[230:231], %19 offset:11520\n\t" \
  "ds_read_b64_tr_b16 v[232:233], %20 offset:10240\n\t" \
  "v_mfma_f32_16x16x32_bf16 %2, v[212:215], v[208:211], %2\n\t" \
  "ds_read_b64_tr_b16 v[234:235], %20 offset:11520\n\t" \
  "ds_read_b64_tr_b16 v[236:237], %21 offset:10240\n\t" \
  "v_mfma_f32_16x16x32_bf16 %3, v[216:219], v[200:203], %3\n\t" \
  "ds_read_b64_tr_b16 v[238:239], %21 offset:11520\n\t" \
  "ds_read_b128 v[244:247], %23 offset:2048\n\t" \
  "v_mfma_f32_16x16x32_bf16 %4, v[216:219], v[204:207], %4\n\t" \
  "ds_read_b128 v[248:251], %23 offset:4096\n\t" \
  "ds_read_b128 v[252:255], %23 offset:6144\n\t" \
  "v_mfma_f32_16x16x32_bf16 %5, v[216:219], v[208:211], %5\n\t" \
  "v_add_u32 %19, %24, %19\n\t" \
  "v_mfma_f32_16x16x32_bf16 %6, v[220:223], v[200:203], %6\n\t" \
  "v_add_u32 %20, %24, %20\n\t" \
  "v_mfma_f32_16x16x32_bf16 %7, v[220:223], v[204:207], %7\n\t" \
  "v_add_u32 %21, %24, %21\n\t" \
  "v_mfma_f32_16x16x32_bf16 %8, v[220:223], v[208:211], %8\n\t" \
  "v_add_u32 %22, %24, %22\n\t" \
  "v_mfma_f32_16x16x32_bf16 %9, v[224:227], v[200:203], %9\n\t" \
  "v_add_u32 %23, %24, %23\n\t" \
  "v_mfma_f32_16x16x32_bf16 %10, v[224:227], v[204:207], %10\n\t" \
  "v_mfma_f32_16x16x32_bf16 %11, v[224:227], v[208:211], %11\n\t" \
  "s_waitcnt lgkmcnt(0)\n\t" \
  "s_barrier\n\t" \
  "v_mfma_f32_16x16x32_bf16 %0, v[240:243], v[228:231], %0\n\t" \
  "ds_read_b128 v[212:215], %22 offset:0\n\t" \
  "ds_read_b64_tr_b16 v[200:201], %19 offset:0\n\t" \
  "v_mfma_f32_16x16x32_bf16 %1, v[240:243], v[232:235], %1\n\t" \
  "ds_read_b64_tr_b16 v[202:203], %19 offset:1280\n\t" \
  "ds_read_b64_tr_b16 v[204:205], %20 offset:0\n\t" \
  "v_mfma_f32_16x16x32_bf16 %2, v[240:243], v[236:239], %2\n\t" \
  "ds_read_b64_tr_b16 v[206:207], %20 offset:1280\n\t" \
  "ds_read_b64_tr_b16 v[208:209], %21 offset:0\n\t" \
  "v_mfma_f32_16x16x32_bf16 %3, v[244:247], v[228:231], %3\n\t" \
  "ds_read_b64_tr_b16 v[210:211], %21 offset:1280\n\t" \
  "ds_read_b128 v[216:219], %22 offset:2048\n\t" \
  "v_mfma_f32_16x16x32_bf16 %4, v[244:247], v[232:235], %4\n\t" \
  "ds_read_b128 v[220:223], %22 offset:4096\n\t" \
  "ds_read_b128 v[224:227], %22 offset:6144\n\t" \
  "v_mfma_f32_16x16x32_bf16 %5, v[244:247], v[236:239], %5\n\t" \
  "v_mfma_f32_16x16x32_bf16 %6, v[248:251], v[228:231], %6\n\t" \
  "v_mfma_f32_16x16x32_bf16 %7, v[248:251], v[232:235], %7\n\t" \
  "v_mfma_f32_16x16x32_bf16 %8, v[248:251], v[236:239], %8\n\t" \
  "v_mfma_f32_16x16x32_bf16 %9, v[252:255], v[228:231], %9\n\t" \
  "v_mfma_f32_16x16x32_bf16 %10, v[252:255], v[232:235], %10\n\t" \
  "v_mfma_f32_16x16x32_bf16 %11, v[252:255], v[236:239], %11\n\t" \
  "s_waitcnt lgkmcnt(0)\n\t" \
  "s_nop 7\n\t"

#define ASM_CHUNK_B0 \
  "s_nop 1\n\t" \
  "s_waitcnt lgkmcnt(0)\n\t" \
  "v_mfma_f32_16x16x32_bf16 %0, v[212:215], v[200:203], 0\n\t" \
  "ds_read_b128 v[240:243], %23 offset:0\n\t" \
  "ds_read_b64_tr_b16 v[228:229], %19 offset:10240\n\t" \
  "v_mfma_f32_16x16x32_bf16 %1, v[212:215], v[204:207], 0\n\t" \
  "ds_read_b64_tr_b16 v[230:231], %19 offset:11520\n\t" \
  "ds_read_b64_tr_b16 v[232:233], %20 offset:10240\n\t" \
  "v_mfma_f32_16x16x32_bf16 %2, v[212:215], v[208:211], 0\n\t" \
  "ds_read_b64_tr_b16 v[234:235], %20 offset:11520\n\t" \
  "ds_read_b64_tr_b16 v[236:237], %21 offset:10240\n\t" \
  "v_mfma_f32_16x16x32_bf16 %3, v[216:219], v[200:203], 0\n\t" \
  "ds_read_b64_tr_b16 v[238:239], %21 offset:11520\n\t" \
  "ds_read_b128 v[244:247], %23 offset:2048\n\t" \
  "v_mfma_f32_16x16x32_bf16 %4, v[216:219], v[204:207], 0\n\t" \
  "ds_read_b128 v[248:251], %23 offset:4096\n\t" \
  "ds_read_b128 v[252:255], %23 offset:6144\n\t" \
  "v_mfma_f32_16x16x32_bf16 %5, v[216:219], v[208:211], 0\n\t" \
  "v_add_u32 %19, %24, %19\n\t" \
  "v_mfma_f32_16x16x32_bf16 %6, v[220:223], v[200:203], 0\n\t" \
  "v_add_u32 %20, %24, %20\n\t" \
  "v_mfma_f32_16x16x32_bf16 %7, v[220:223], v[204:207], 0\n\t" \
  "v_add_u32 %21, %24, %21\n\t" \
  "v_mfma_f32_16x16x32_bf16 %8, v[220:223], v[208:211], 0\n\t" \
  "v_add_u32 %22, %24, %22\n\t" \
  "v_mfma_f32_16x16x32_bf16 %9, v[224:227], v[200:203], 0\n\t" \
  "v_add_u32 %23, %24, %23\n\t" \
  "v_mfma_f32_16x16x32_bf16 %10, v[224:227], v[204:207], 0\n\t" \
  "v_mfma_f32_16x16x32_bf16 %11, v[224:227], v[208:211], 0\n\t" \
  "s_waitcnt lgkmcnt(0)\n\t" \
  "s_barrier\n\t" \
  "v_mfma_f32_16x16x32_bf16 %0, v[240:243], v[228:231], %0\n\t" \
  "ds_read_b128 v[212:215], %22 offset:0\n\t" \
  "ds_read_b64_tr_b16 v[200:201], %19 offset:0\n\t" \
  "v_mfma_f32_16x16x32_bf16 %1, v[240:243], v[232:235], %1\n\t" \
  "ds_read_b64_tr_b16 v[202:203], %19 offset:1280\n\t" \
  "ds_read_b64_tr_b16 v[204:205], %20 offset:0\n\t" \
  "v_mfma_f32_16x16x32_bf16 %2, v[240:243], v[236:239], %2\n\t" \
  "ds_read_b64_tr_b16 v[206:207], %20 offset:1280\n\t" \
  "ds_read_b64_tr_b16 v[208:209], %21 offset:0\n\t" \
  "v_mfma_f32_16x16x32_bf16 %3, v[244:247], v[228:231], %3\n\t" \
  "ds_read_b64_tr_b16 v[210:211], %21 offset:1280\n\t" \
  "ds_read_b128 v[216:219], %22 offset:2048\n\t" \
  "v_mfma_f32_16x16x32_bf16 %4, v[244:247], v[232:235], %4\n\t" \
  "ds_read_b128 v[220:223], %22 offset:4096\n\t" \
  "ds_read_b128 v[224:227], %22 offset:6144\n\t" \
  "v_mfma_f32_16x16x32_bf16 %5, v[244:247], v[236:239], %5\n\t" \
  "v_mfma_f32_16x16x32_bf16 %6, v[248:251], v[228:231], %6\n\t" \
  "v_mfma_f32_16x16x32_bf16 %7, v[248:251], v[232:235], %7\n\t" \
  "v_mfma_f32_16x16x32_bf16 %8, v[248:251], v[236:239], %8\n\t" \
  "v_mfma_f32_16x16x32_bf16 %9, v[252:255], v[228:231], %9\n\t" \
  "v_mfma_f32_16x16x32_bf16 %10, v[252:255], v[232:235], %10\n\t" \
  "v_mfma_f32_16x16x32_bf16 %11, v[252:255], v[236:239], %11\n\t" \
  "s_waitcnt lgkmcnt(0)\n\t" \
  "s_nop 7\n\t"

#define ASM_LOAD_A \
  "ds_read_b64_tr_b16 v[200:201], %7 offset:0\n\t" \
  "ds_read_b64_tr_b16 v[202:203], %7 offset:1536\n\t" \
  "ds_read_b128 v[212:215], %10 offset:0\n\t" \
  "ds_read_b128 v[216:219], %10 offset:2048\n\t" \
  "ds_read_b128 v[220:223], %10 offset:4096\n\t" \
  "ds_read_b128 v[224:227], %10 offset:6144\n\t" \
  "ds_read_b64_tr_b16 v[204:205], %8 offset:0\n\t" \
  "ds_read_b64_tr_b16 v[206:207], %8 offset:1536\n\t" \
  "ds_read_b64_tr_b16 v[208:209], %9 offset:0\n\t" \
  "ds_read_b64_tr_b16 v[210:211], %9 offset:1536\n\t"

#define ASM_LOAD_B \
  "ds_read_b128 v[212:215], %10 offset:0\n\t" \
  "ds_read_b64_tr_b16 v[200:201], %7 offset:0\n\t" \
  "ds_read_b64_tr_b16 v[202:203], %7 offset:1280\n\t" \
  "ds_read_b64_tr_b16 v[204:205], %8 offset:0\n\t" \
  "ds_read_b64_tr_b16 v[206:207], %8 offset:1280\n\t" \
  "ds_read_b64_tr_b16 v[208:209], %9 offset:0\n\t" \
  "ds_read_b64_tr_b16 v[210:211], %9 offset:1280\n\t" \
  "ds_read_b128 v[216:219], %10 offset:2048\n\t" \
  "ds_read_b128 v[220:223], %10 offset:4096\n\t" \
  "ds_read_b128 v[224:227], %10 offset:6144\n\t"

struct Geo { unsigned b, g, j, h, uo, d0, hl; };
DI unsigned head_at(const Geo& G, unsigned u) { return (G.j + (G.hl >> (2 * u))) & 3; }

struct ChunkOrder {
  unsigned used, p1, p2, off;
  DI ChunkOrder(const Geo& G) {
    p1 = 1 + (G.b & 3); p2 = 5 + (G.b >= 5 ? G.b - 5 : G.b);
    used = 1u | (1u << p1) | (1u << p2);
    off = (G.j * 10 + 2) >> 2;
  }
  DI unsigned next(unsigned n) {
    unsigned r;
    if (n == 0) r = 0; else if (n == 1) r = p1; else if (n == 2) r = p2;
    else { r = __builtin_ctz(~used); used |= 1u << r; }
    r += off;
    return r >= 10 ? r - 10 : r;
  }
};
DI void dma_wave_w(const Geo& G, unsigned dw, const void* kc, const void* wq, const void* wo, unsigned lane) {
  const __amdgpu_buffer_rsrc_t rA = __builtin_amdgcn_make_buffer_rsrc((void*)wq, 0, DM * NH * DH * 2, 0x00020000);
  unsigned vA[6], rr[3], cc[3];
#pragma unroll
  for (int m = 0; m < 3; ++m) {
    const unsigned u = 64 * m + lane, r = (u * 2731) >> 16, c = u - 24 * r;
    rr[m] = r; cc[m] = c;
    vA[m] = r * 12288 + ((c ^ (r & 2)) << 4);
    vA[m + 3] = (vA[m] ^ 64) + 8 * 12288;
  }
  ChunkOrder ord(G);
  auto issueA = [&](unsigned n) {
    const unsigned kcx = ord.next(n);
    const unsigned slot = (n & 3) * SLOT + dw * 12288, so = (kcx * 64 + dw * 32) * 12288 + G.h * 384;
#pragma unroll
    for (int p = 0; p < 12; ++p) dma16(rA, slot + p * 1024, vA[p % 6], so + (p / 6) * 16 * 12288);
  };
  issueA(0); issueA(1);
  WAITVM(12); BAR();
  const __amdgpu_buffer_rsrc_t rB = __builtin_amdgcn_make_buffer_rsrc((void*)wo, 0, DM * NH * DH * 2, 0x00020000);
  unsigned vB[5];
#pragma unroll
  for (int m = 0; m < 5; ++m) {
    const unsigned u = 64 * m + lane, r = (u * 3277) >> 16, c = u - 20 * r;
    vB[m] = r * 1280 + ((c ^ ((r >> 2) & 2)) << 4);
  }
  unsigned hl = G.hl, dc = G.d0, o3 = 0;
  const unsigned sb = (768 * G.g + dw * 32) * 1280 + 320 * G.j;
  auto issueB = [&](unsigned n) {
    const unsigned hh = (G.j + hl) & 3;
    const unsigned slot = (n & 3) * SLOT + dw * 10240, so = sb + (192 * hh + 64 * dc) * 1280;
#pragma unroll
    for (int p = 0; p < 10; ++p) dma16(rB, slot + p * 1024, vB[p % 5], so + (p / 5) * 16 * 1280);
    dc = dc == 2 ? 0u : dc + 1;
    hl = o3 == 2 ? hl >> 2 : hl;
    o3 = o3 == 2 ? 0u : o3 + 1;
  };
  {
    const __amdgpu_buffer_rsrc_t rK = __builtin_amdgcn_make_buffer_rsrc((void*)kc, 0, NB * NH * SK * DH * 2, 0x00020000);
    const unsigned so = ((G.b * 32 + G.h) * 64 + dw * 32) * 384;
    unsigned vK[3];
#pragma unroll
    for (int m = 0; m < 3; ++m) vK[m] = rr[m] * 384 + ((cc[m] ^ (rr[m] >> 1)) << 4);
    issueA(2); WAITVM(12); BAR();
    issueA(3); WAITVM(12); BAR();
#pragma unroll
    for (int t = 2; t < 8; ++t) {
      issueA(t + 2);
#pragma unroll
      for (int p = 2 * (t - 2); p < 2 * (t - 2) + 2; ++p)
        dma16(rK, PBUF + dw * 12288 + p * 1024, (p % 6) < 3 ? vK[p % 3] : (vK[p % 3] ^ 64) + 8 * 384, so + (p / 6) * 16 * 384);
      if (t == 2) WAITVM(14); else WAITVM(16);
      BAR();
    }
  }
  issueB(10); WAITVM(12); BAR();
  issueB(11); WAITVM(20); BAR();
  WAITVM(10); BAR();
  issueB(12); WAITVM(10); BAR();
#pragma nounroll
  for (unsigned t = 12; t < 21; ++t) { issueB(t + 1); WAITVM(10); BAR(); }
  WAITVM(0); BAR();
  BAR();
}
DI void dma_wave_a(const Geo& G, const void* x, const void* vc, unsigned lane) {
  const __amdgpu_buffer_rsrc_t rA = __builtin_amdgcn_make_buffer_rsrc((void*)x, 0, NB * SQ * DM * 2, 0x00020000);
  const unsigned r = lane >> 3, c = lane & 7, cs = (c ^ (r >> 1)) << 4;
  const unsigned vA0 = r * 1280 + cs, vA1 = r * 1280 + (cs ^ 64);
  ChunkOrder ord(G);
  auto issueA = [&](unsigned n) {
    const unsigned kcx = ord.next(n);
    const unsigned slot = (n & 3) * SLOT + AUX, so = G.b * 64 * 1280 + kcx * 128;
#pragma unroll
    for (int p = 0; p < 8; ++p) dma16(rA, slot + p * 1024, (p & 1) ? vA1 : vA0, so + p * 8 * 1280);
  };
  issueA(0); issueA(1);
  WAITVM(8);
  BAR();
  const __amdgpu_buffer_rsrc_t rB = __builtin_amdgcn_make_buffer_rsrc((void*)vc, 0, NB * NH * SK * DH * 2, 0x00020000);
  const unsigned vB0 = r * 384 + cs, vB1 = r * 384 + (cs ^ 64);
  unsigned hl = G.hl, dc = G.d0, o3 = 0;
  auto issueB = [&](unsigned n) {
    const unsigned hh = (G.j + hl) & 3;
    const unsigned slot = (n & 3) * SLOT + AUX, so = (G.b * 32 + 4 * G.g + hh) * 24576 + dc * 128;
#pragma unroll
    for (int p = 0; p < 8; ++p) dma16(rB, slot + p * 1024, (p & 1) ? vB1 : vB0, so + p * 8 * 384);
    dc = dc == 2 ? 0u : dc + 1;
    hl = o3 == 2 ? hl >> 2 : hl;
    o3 = o3 == 2 ? 0u : o3 + 1;
  };
  issueA(2); WAITVM(8); BAR();
#pragma nounroll
  for (unsigned t = 1; t < 9; ++t) { if (t < 8) issueA(t + 2); else issueB(t + 2); WAITVM(8); BAR(); }
  issueB(11); BAR();
  WAITVM(8); BAR();
  issueB(12); WAITVM(8); BAR();
#pragma nounroll
  for (unsigned t = 12; t < 21; ++t) { issueB(t + 1); WAITVM(8); BAR(); }
  WAITVM(0); BAR();
  BAR();
}

DI void reduce_tile(const Geo& G, unsigned dt, u64 tagR, char* ws, void* out, unsigned lane, bool upd) {
  const unsigned nt = dt >> 1, ip = dt & 1;
  const unsigned pw = nt < 3 ? 0u : nt < 6 ? 1u : nt < 8 ? 2u : 3u;
  const char* fl = ws + WS_FR + (size_t)((((G.b * 4 + G.j) * 8 + (lane & 7)) * 4 + pw) * 128);
#pragma nounroll
  for (unsigned sp = 0; sp < SPIN_CAP; ++sp) {
    u64 v = ld_sc1_64(fl);
    if (__all(v == tagR)) break;
  }
  CBAR();
  __amdgpu_buffer_rsrc_t rR = __builtin_amdgcn_make_buffer_rsrc((void*)(ws + WS_R), 0, 256 * 20480, 0x00020000);
  const unsigned base = ((G.b * 4 + G.j) * 8) * 20480 + dt * 1024;
  u32x4 v[8];
#pragma unroll
  for (int s = 0; s < 8; ++s) v[s] = __builtin_amdgcn_raw_buffer_load_b128(rR, lane * 16, base + s * 20480, SC1);
  float a[8];
#pragma unroll
  for (int k = 0; k < 8; ++k) a[k] = 0.f;
  const bf16x2 e0 = __builtin_bit_cast(bf16x2, opaque(0x00003f80u)), e1 = __builtin_bit_cast(bf16x2, opaque(0x3f800000u));
#pragma unroll
  for (int s = 0; s < 8; ++s)
#pragma unroll
    for (int k = 0; k < 4; ++k) {
      const unsigned wv = v[s][k];
      const bf16x2 x2 = __builtin_bit_cast(bf16x2, wv);
      a[2 * k] = __builtin_amdgcn_fdot2_f32_bf16(x2, e0, a[2 * k], false);
      a[2 * k + 1] = __builtin_amdgcn_fdot2_f32_bf16(x2, e1, a[2 * k + 1], false);
    }
  __amdgpu_buffer_rsrc_t rO = __builtin_amdgcn_make_buffer_rsrc(out, 0, NB * SQ * DM * 2, 0x00020000);
  const unsigned row = G.b * 64 + 32 * ip + (lane & 15), col = 160 * G.j + 16 * nt + 4 * (lane >> 4);
  u32x2 o0 = {cvtpk(a[0], a[1]), cvtpk(a[2], a[3])}, o1 = {cvtpk(a[4], a[5]), cvtpk(a[6], a[7])};
  __builtin_amdgcn_raw_buffer_store_b64(o0, rO, (row * DM + col) * 2, 0, 0);
  __builtin_amdgcn_raw_buffer_store_b64(o1, rO, ((row + 16) * DM + col) * 2, 0, 0);
  if (upd && lane == 0) st_sc1_64(ws + WS_ER + (G.b * 4 + G.j) * 128, tagR);
}

struct Frag { u32x4 f[7]; };
#define F0_OPS(F) "+{v[200:203]}"(F.f[0]), "+{v[204:207]}"(F.f[1]), "+{v[208:211]}"(F.f[2]), "+{v[212:215]}"(F.f[3]), "+{v[216:219]}"(F.f[4]), "+{v[220:223]}"(F.f[5]), "+{v[224:227]}"(F.f[6])
#define F1_CLOB "v228", "v229", "v230", "v231", "v232", "v233", "v234", "v235", "v236", "v237", "v238", "v239", "v240", "v241", "v242", "v243", "v244", "v245", "v246", "v247", "v248", "v249", "v250", "v251", "v252", "v253", "v254", "v255"
#define RUN_CHUNK(TXT, acc, F, ad, delta) asm volatile(TXT \
    : "+v"(acc[0]), "+v"(acc[1]), "+v"(acc[2]), "+v"(acc[3]), "+v"(acc[4]), "+v"(acc[5]), "+v"(acc[6]), "+v"(acc[7]), "+v"(acc[8]), "+v"(acc[9]), "+v"(acc[10]), "+v"(acc[11]), \
      F0_OPS(F), "+v"(ad[0]), "+v"(ad[1]), "+v"(ad[2]), "+v"(ad[3]), "+v"(ad[4]) : "s"(delta) : F1_CLOB, "memory")
#define RUN_CHUNK0(TXT, acc, F, ad, delta) asm volatile(TXT \
    : "=&v"(acc[0]), "=&v"(acc[1]), "=&v"(acc[2]), "=&v"(acc[3]), "=&v"(acc[4]), "=&v"(acc[5]), "=&v"(acc[6]), "=&v"(acc[7]), "=&v"(acc[8]), "=&v"(acc[9]), "=&v"(acc[10]), "=&v"(acc[11]), \
      F0_OPS(F), "+v"(ad[0]), "+v"(ad[1]), "+v"(ad[2]), "+v"(ad[3]), "+v"(ad[4]) : "s"(delta) : F1_CLOB, "memory")
#define RUN_LOAD(TXT, F, ad) asm volatile(TXT : F0_OPS(F) : "v"(ad[0]), "v"(ad[1]), "v"(ad[2]), "v"(ad[3]) : "memory")
DI unsigned slot_delta(unsigned c) { return (c & 3) == 3 ? (unsigned)(-3 * (int)SLOT) : SLOT; }

__global__ void __launch_bounds__(512, 2)
mha_fused(const void* __restrict__ x, const void* __restrict__ kc, const void* __restrict__ vc, const void* __restrict__ wq,
          const void* __restrict__ wo, void* __restrict__ out, char* __restrict__ ws) {
  const unsigned tid = threadIdx.x, lane = tid & 63, wave = rfl(tid >> 6);
  Geo G;
  { const unsigned bid = blockIdx.x; G.g = bid & 7; const unsigned idx = bid >> 3; G.j = idx & 3; G.b = idx >> 2; G.h = 4 * G.g + G.j;
    G.uo = G.b >= 2 ? 1u : 0u; G.d0 = G.b & 1;
    G.hl = (0xE1D293E4u >> (8 * (G.b >> 1))) & 0xffu; }
  if (__builtin_expect(wave >= 4 && wave < 7, 1)) {
    if (__builtin_expect(wave < 6, 1)) dma_wave_w(G, wave - 4, kc, wq, wo, lane);
    else dma_wave_a(G, x, vc, lane);
    const u64 tagR = lds_u64(MISC + L_TAGR);
    const unsigned dt = G.g + 8 * (wave - 4);
    if (dt < 20) reduce_tile(G, dt, tagR, ws, out, lane, wave == 4 && G.g == 0);
  } else if (wave == 7) {
    u64 eP, eR;
    asm volatile("global_load_dwordx2 %0, %1, off sc1" : "=v"(eP) : "v"(ws + WS_EP + (G.b * 8 + G.g) * 128) : "memory");
    asm volatile("global_load_dwordx2 %0, %1, off sc1" : "=v"(eR) : "v"(ws + WS_ER + (G.b * 4 + G.j) * 128) : "memory");
    BAR(); BAR(); BAR(); BAR(); BAR(); BAR(); BAR();
    asm volatile("s_waitcnt vmcnt(0)" : "+v"(eP), "+v"(eR) :: "memory");
    const u64 tagP = next_tag(eP), tagR = next_tag(eR);
    if (lane == 0) { lds_st32(MISC + L_TAGP, (unsigned)tagP); lds_st32(MISC + L_TAGP + 4, (unsigned)(tagP >> 32));
                     lds_st32(MISC + L_TAGR, (unsigned)tagR); lds_st32(MISC + L_TAGR + 4, (unsigned)(tagR >> 32)); }
    BAR(); asm volatile("s_waitcnt lgkmcnt(0)" ::: "memory"); BAR(); BAR(); BAR();
    BAR(); BAR(); BAR();
    const __amdgpu_buffer_rsrc_t rP = __builtin_amdgcn_make_buffer_rsrc((void*)(ws + WS_P), 0, 256 * 8192, 0x00020000);
    const char* fl = ws + WS_FP + (size_t)((G.b * 32 + 4 * G.g) * 4 + (lane & 15)) * 128;
    unsigned s = 13;
    {
      u64 pv;
      asm volatile("global_load_dwordx2 %0, %1, off sc1" : "=v"(pv) : "v"(fl) : "memory");
#pragma nounroll
      for (unsigned sp = 0; sp < SPIN_CAP; ++sp) {
        if (s == 20 || vm_idle()) {
          asm volatile("s_waitcnt vmcnt(0)" : "+v"(pv) :: "memory");
          if (__all(pv == tagP)) break;
          asm volatile("global_load_dwordx2 %0, %1, off sc1" : "=v"(pv) : "v"(fl) : "memory");
        }
        if (s < 20) { BAR(); ++s; }
      }
      asm volatile("s_waitcnt vmcnt(0)" : "+v"(pv) :: "memory");
    }
    auto fetchP = [&](unsigned u, unsigned buf) {
      const unsigned hh = head_at(G, u);
#pragma unroll
      for (int i = 0; i < 8; ++i) {
        const unsigned dst = buf == 3 ? (i >> 2) * SLOT + 20480 + (i & 3) * 1024 : PBUF + (i >> 1) * 6144 + buf * 2048 + (i & 1) * 1024;
        __builtin_amdgcn_raw_ptr_buffer_load_lds(rP, (LAS void*)(uintptr_t)dst, 16, lane * 16, (G.b * 32 + 4 * G.g + hh) * 8192 + i * 1024, 0, SC1);
      }
    };
    fetchP(G.uo == 0 ? 1u : 0u, 1);
    fetchP(2, 2);
    fetchP(3, 3);
    if (G.j == 0 && lane == 0) st_sc1_64(ws + WS_EP + (G.b * 8 + G.g) * 128, tagP);
#pragma nounroll
    while (s < 20) { BAR(); ++s; }
    WAITVM(0);
    BAR(); ++s;
    BAR(); BAR();
  } else {
    const unsigned i16 = lane & 15, g4 = lane >> 4, q = i16 >> 2, p = lane & 3;
    const unsigned a0 = AUX + i16 * 128 + ((g4 ^ (i16 >> 1)) << 4);
    Frag F0;
#pragma unroll
    for (int i = 0; i < 7; ++i) F0.f[i] = (u32x4){0, 0, 0, 0};
    unsigned ad[5];
    f32x4 acc[12];
#pragma unroll
    for (int t = 0; t < 12; ++t) acc[t] = (f32x4){0.f, 0.f, 0.f, 0.f};
    { const unsigned phi = ((q >> 1) & 1) | ((g4 & 1) << 1);
#pragma unroll
      for (int e = 0; e < 3; ++e) ad[e] = (8 * g4 + q) * 384 + 32 * ((3 * wave + e) ^ phi) + 16 * (p >> 1) + 8 * (p & 1);
      ad[3] = a0; ad[4] = a0 ^ 64; }
    asm volatile("" : "+v"(acc[0]), "+v"(acc[1]), "+v"(acc[2]), "+v"(acc[3]), "+v"(acc[4]), "+v"(acc[5]), "+v"(acc[6]), "+v"(acc[7]), "+v"(acc[8]), "+v"(acc[9]), "+v"(acc[10]), "+v"(acc[11]),
                      "+v"(ad[0]), "+v"(ad[1]), "+v"(ad[2]), "+v"(ad[3]), "+v"(ad[4]));
    BAR();
    RUN_LOAD(ASM_LOAD_A, F0, ad);
#pragma nounroll
    for (unsigned c = 0; c < 10; ++c) { const unsigned dl = slot_delta(c); RUN_CHUNK(ASM_CHUNK_A, acc, F0, ad, dl); }
    bf16x8 kf[6][4];
    {
      const unsigned fk = i16 >> 1;
      unsigned ka[2];
#pragma unroll
      for (int so = 0; so < 2; ++so) ka[so] = opaque(PBUF + i16 * 384 + (((4 * so + g4) ^ fk) << 4));
#pragma unroll
      for (int s = 0; s < 6; ++s)
#pragma unroll
        for (int kt = 0; kt < 4; ++kt) kf[s][kt] = lds_b128(ka[s & 1] + kt * 16 * 384 + 128 * (s >> 1));
    }
    {
      const unsigned qw = opaque(SLOT + ((g4 >> 1) * 16 + i16) * 16 + (g4 & 1) * 8);
#pragma unroll
      for (int e = 0; e < 3; ++e) {
        const unsigned tau = 3 * wave + e;
        const unsigned qe = qw + (tau >> 1) * 1024 + (tau & 1) * 512;
#pragma unroll
        for (int t = 0; t < 4; ++t) { u32x2 v = {cvtpk(acc[4 * e + t][0], acc[4 * e + t][1]), cvtpk(acc[4 * e + t][2], acc[4 * e + t][3])};
          *(LAS u32x2*)(uintptr_t)(qe + t * 6144) = v; }
      }
    }
    const unsigned ntb = wave == 3 ? 7u : 3u * wave;
    { const unsigned slot = (10 & 3) * SLOT;
#pragma unroll
      for (int e = 0; e < 3; ++e) ad[e] = slot + (8 * g4 + q) * 320 + 32 * ((ntb + e) ^ (g4 & 1)) + 16 * (p >> 1) + 8 * (p & 1);
      ad[3] = slot + a0; ad[4] = slot + (a0 ^ 64); }
    const unsigned qr = opaque(SLOT + wave * 6144 + lane * 16);
    BAR_L();
    f32x4 st[4];
    {
      bf16x8 qf[6];
#pragma unroll
      for (int s = 0; s < 6; ++s) qf[s] = lds_b128(qr + s * 1024);
      RUN_LOAD(ASM_LOAD_B, F0, ad);
      SB0();
#pragma unroll
      for (int kt = 0; kt < 4; ++kt) st[kt] = (f32x4){0.f, 0.f, 0.f, 0.f};
#pragma unroll
      for (int s = 0; s < 6; ++s)
#pragma unroll
        for (int kt = 0; kt < 4; ++kt) st[kt] = MFMA(kf[s][kt], qf[s], st[kt]);
    }
    SB0();
    asm volatile("s_nop 7" : "+v"(st[0]), "+v"(st[1]), "+v"(st[2]), "+v"(st[3]));
    const __amdgpu_buffer_rsrc_t rP = __builtin_amdgcn_make_buffer_rsrc((void*)(ws + WS_P), 0, 256 * 8192, 0x00020000);
    {
#pragma unroll
      for (int kt = 0; kt < 4; ++kt)
#pragma unroll
        for (int r = 0; r < 4; r += 2) {
          unsigned u = cvtpk(st[kt][r], st[kt][r + 1]);
          u = cvtpk(__uint_as_float(u << 16) * 0.07216878364870322f, __uint_as_float(u & 0xffff0000u) * 0.07216878364870322f);
          st[kt][r] = __uint_as_float(u << 16); st[kt][r + 1] = __uint_as_float(u & 0xffff0000u);
        }
      float m = max3f(st[0][0], st[0][1], st[0][1]);
      m = max3f(m, st[0][2], st[0][3]);
#pragma unroll
      for (int kt = 1; kt < 4; ++kt) { m = max3f(m, st[kt][0], st[kt][1]); m = max3f(m, st[kt][2], st[kt][3]); }
      { auto r1 = __builtin_amdgcn_permlane16_swap(__float_as_uint(m), __float_as_uint(m), false, false);
        m = max3f(__uint_as_float(r1[0]), __uint_as_float(r1[1]), __uint_as_float(r1[1]));
        auto r2 = __builtin_amdgcn_permlane32_swap(__float_as_uint(m), __float_as_uint(m), false, false);
        m = max3f(__uint_as_float(r2[0]), __uint_as_float(r2[1]), __uint_as_float(r2[1])); }
      constexpr float SC = 1.4426950408889634f;
      const float ml = m * SC;
      float sum = 0.f;
#pragma unroll
      for (int kt = 0; kt < 4; ++kt)
#pragma unroll
        for (int r = 0; r < 4; ++r) { const float e = __builtin_amdgcn_exp2f(__builtin_fmaf(st[kt][r], SC, -ml)); st[kt][r] = e; sum += e; }
      { auto r1 = __builtin_amdgcn_permlane16_swap(__float_as_uint(sum), __float_as_uint(sum), false, false);
        sum = __uint_as_float(r1[0]) + __uint_as_float(r1[1]);
        auto r2 = __builtin_amdgcn_permlane32_swap(__float_as_uint(sum), __float_as_uint(sum), false, false);
        sum = __uint_as_float(r2[0]) + __uint_as_float(r2[1]); }
      const float inv = __builtin_amdgcn_rcpf(sum);
#pragma unroll
      for (int kt = 0; kt < 4; ++kt) st[kt] *= inv;
      const unsigned pw = opaque(PBUF + wave * 6144 + lane * 16);
#pragma unroll
      for (int s2 = 0; s2 < 2; ++s2) {
        const u32x4 pf = __builtin_bit_cast(u32x4, pack8(st[2 * s2], st[2 * s2 + 1]));
        __builtin_amdgcn_raw_buffer_store_b128(pf, rP, lane * 16, (G.b * 32 + G.h) * 8192 + (2 * wave + s2) * 1024, SC1);
        *(LAS u32x4*)(uintptr_t)(pw + s2 * 1024) = pf;
      }
    }
    f32x4 oa[4][3];
#pragma unroll
    for (int it = 0; it < 4; ++it)
#pragma unroll
      for (int e = 0; e < 3; ++e) oa[it][e] = (f32x4){0.f, 0.f, 0.f, 0.f};
    auto pvw = [&](unsigned buf, const bf16x8 (&pk)[2][3]) {
      const unsigned pb = opaque((buf == 3 ? 20480u : PBUF + buf * 2048) + lane * 16);
      bf16x8 pf[4][2];
#pragma unroll
      for (int s2 = 0; s2 < 2; ++s2)
#pragma unroll
        for (int it = 0; it < 4; ++it) pf[it][s2] = lds_b128(pb + (buf == 3 ? (it >> 1) * SLOT + (it & 1) * 2048 : it * 6144) + s2 * 1024);
      SB0();
#pragma unroll
      for (int s2 = 0; s2 < 2; ++s2)
#pragma unroll
        for (int it = 0; it < 4; ++it)
#pragma unroll
          for (int e = 0; e < 3; ++e) oa[it][e] = MFMA(pk[s2][e], pf[it][s2], oa[it][e]);
      SB0();
    };
    f32x4 vw[12];
    auto pack_vw = [&](bf16x8 (&pk)[2][3]) {
#pragma unroll
      for (int s2 = 0; s2 < 2; ++s2)
#pragma unroll
        for (int e = 0; e < 3; ++e) pk[s2][e] = pack8(vw[3 * (2 * s2) + e], vw[3 * (2 * s2 + 1) + e]);
    };
    auto head_first = [&](unsigned c) { const unsigned dl = slot_delta(c); RUN_CHUNK0(ASM_CHUNK_B0, vw, F0, ad, dl); };
    auto head_next = [&](unsigned c) { const unsigned dl = slot_delta(c); RUN_CHUNK(ASM_CHUNK_B, vw, F0, ad, dl); };
    bf16x8 pk1[2][3], pk2[2][3];
    head_first(10);
    head_next(11);
    {
      const u64 tagP = lds_u64(MISC + L_TAGP);
      WAITVM(0);
      if (lane == 0) st_sc1_64(ws + WS_FP + (size_t)((G.b * 32 + G.h) * 4 + wave) * 128, tagP); }
    head_next(12);
    pack_vw(pk1);
    if (G.uo == 0) pvw(0, pk1);
    head_first(13); head_next(14); head_next(15);
    pack_vw(pk2);
    if (G.uo == 1) pvw(0, pk2);
    else {
#pragma unroll
      for (int s2 = 0; s2 < 2; ++s2)
#pragma unroll
        for (int e = 0; e < 3; ++e) pk1[s2][e] = pk2[s2][e];
    }
    head_first(16); head_next(17); head_next(18);
    pack_vw(pk2);
    head_first(19);
    pvw(1, pk1);
    head_next(20);
    pvw(2, pk2);
    head_next(21);
    pack_vw(pk1); pvw(3, pk1);
    const u64 tagR = lds_u64(MISC + L_TAGR);
    __amdgpu_buffer_rsrc_t rR = __builtin_amdgcn_make_buffer_rsrc((void*)(ws + WS_R), 0, 256 * 20480, 0x00020000);
    const unsigned base = ((G.b * 4 + G.j) * 8 + G.g) * 20480;
#pragma unroll
    for (int e = 0; e < 3; ++e) {
      const bool keep = wave < 2 || (wave == 2 ? e < 2 : e > 0);
      if (keep) {
#pragma unroll
        for (int ip = 0; ip < 2; ++ip)
          __builtin_amdgcn_raw_buffer_store_b128(__builtin_bit_cast(u32x4, pack8(oa[2 * ip][e], oa[2 * ip + 1][e])), rR, lane * 16, base + (2 * (ntb + e) + ip) * 1024, SC1);
      }
    }
    WAITVM(0);
    if (lane == 0) st_sc1_64(ws + WS_FR + (size_t)((((G.b * 4 + G.j) * 8 + G.g) * 4 + wave) * 128), tagR);
  }
}

extern "C" void kernel_launch(void* const* d_in, const int* in_sizes, int n_in, void* d_out, int out_size, void* d_ws, size_t ws_size,
                              hipStream_t stream) {
  (void)in_sizes; (void)n_in; (void)out_size; (void)ws_size;
  (void)hipFuncSetAttribute((const void*)mha_fused, hipFuncAttributeMaxDynamicSharedMemorySize, LDS_BYTES);
  mha_fused<<<256, 512, LDS_BYTES, stream>>>(d_in[0], d_in[1], d_in[2], d_in[3], d_in[4], d_out, (char*)d_ws);
}
```
